```python
import torch

def _softmax(x, axis=-1):
    return torch.softmax(x, dim=axis)
def _gelu(x):
    return torch.nn.functional.gelu(x, approximate="tanh")
def _silu(x):
    return torch.nn.functional.silu(x)
def _sigmoid(x):
    return torch.sigmoid(x)
def _layernorm(x, axis=-1, eps=1e-5):
    m = x.mean(dim=axis, keepdim=True)
    v = x.var(dim=axis, keepdim=True, unbiased=False)
    return (x - m) / torch.sqrt(v + eps)
def _scatter_add(tgt, idx, src, axis=0):
    return tgt.clone().index_add_(axis, idx, src)
def _segment_sum(src, seg, n):
    out = torch.zeros((n,) + src.shape[1:], dtype=src.dtype)
    return out.index_add_(0, seg, src)
def _pad(x, pw):
    flat = tuple(v for pair in reversed(pw) for v in pair)
    return torch.nn.functional.pad(x, flat)

kernel_name = 'sequential_preLN__mha_block_cached__geglu_ffn__decode'

def setup_inputs():

    _w = (lambda a: (lambda t: t.to(torch.bfloat16) if t.is_floating_point() else t)(torch.as_tensor(a)))
    def _A_setup():
        B, Sq, D = 8, 1, 1024
        H, Sk, Dh = 32, 4096, 128
        return {
            "x": _w((lambda *s: torch.randn(*s, dtype=torch.bfloat16) * 0.05)(B, Sq, D)),
            "k_cache": _w((lambda *s: torch.randn(*s, dtype=torch.bfloat16) * 3.2)(B, H, Sk, Dh)),
            "v_cache": _w((lambda *s: torch.randn(*s, dtype=torch.bfloat16) * 0.4)(B, H, Sk, Dh)),
            "wq": _w((lambda *s: torch.randn(*s, dtype=torch.bfloat16) * 3.2)(D, H * Dh) * (D ** -0.5)),
            "wo": _w((lambda *s: torch.randn(*s, dtype=torch.bfloat16) * 0.4)(H * Dh, D) * ((H * Dh) ** -0.5)),
        }
    _A_in = _A_setup()

    def _B_setup():
        B, S, D, F = 8, 1, 1024, 3584
        return {
            "x": _w((lambda *s: torch.randn(*s, dtype=torch.bfloat16) * 0.05)(B, S, D)),
            "w_gate": _w((lambda *s: torch.randn(*s, dtype=torch.bfloat16) * 0.05)(D, F)),
            "w_up": _w((lambda *s: torch.randn(*s, dtype=torch.bfloat16) * 0.05)(D, F)),
            "w_down": _w((lambda *s: torch.randn(*s, dtype=torch.bfloat16) * 0.05)(F, D)),
        }
    _B_in = _B_setup()

    return {
        "A_x": _A_in["x"],
        "A_k_cache": _A_in["k_cache"],
        "A_v_cache": _A_in["v_cache"],
        "A_wq": _A_in["wq"],
        "A_wo": _A_in["wo"],
        "B_x": _B_in["x"],
        "B_w_gate": _B_in["w_gate"],
        "B_w_up": _B_in["w_up"],
        "B_w_down": _B_in["w_down"],
    }

def reference(A_x, A_k_cache, A_v_cache, A_wq, A_wo, B_x, B_w_gate, B_w_up, B_w_down):
    def _A_ref(x, k_cache, v_cache, wq, wo):
        B, Sq, D = x.shape
        H, Dh = 32, 128
        q = (lambda x, *d: x.permute(*d))((x @ wq).reshape(B, Sq, H, Dh), 0, 2, 1, 3)
        scores = torch.einsum("bhqd,bhkd->bhqk", q, k_cache) * (Dh ** -0.5)
        p = _softmax(scores, axis=-1)
        o = torch.einsum("bhqk,bhkd->bhqd", p, v_cache)
        return (lambda x, *d: x.permute(*d))(o, 0, 2, 1, 3).reshape(B, Sq, H * Dh) @ wo

    def _B_ref(x, w_gate, w_up, w_down):
        return (_gelu(x @ w_gate) * (x @ w_up)) @ w_down

    x = A_x
    h = x + _A_ref(_layernorm(x, axis=-1), A_k_cache, A_v_cache, A_wq, A_wo)
    h2 = h + _B_ref(_layernorm(h, axis=-1), B_w_gate, B_w_up, B_w_down)
    return h2
```

```cpp
#include <hip/hip_runtime.h>
#include <hip/hip_bf16.h>
#include <stdint.h>

#define MIMIC 0

typedef unsigned short bf16_t;
typedef unsigned v4u __attribute__((ext_vector_type(4)));
typedef __bf16 bf16x2 __attribute__((ext_vector_type(2)));

constexpr int B = 8, D = 1024, H = 32, SK = 4096, DH = 128, F = 3584;

__device__ __forceinline__ float bf2f(unsigned short b) { return __builtin_bit_cast(float, (unsigned)b << 16); }
__device__ __forceinline__ unsigned short f2bf(float f) { __bf16 h = (__bf16)f; return __builtin_bit_cast(unsigned short, h); }
__device__ __forceinline__ float rbf(float f) { return bf2f(f2bf(f)); }
__device__ __forceinline__ float rm(float f) { return MIMIC ? rbf(f) : f; }
__device__ __forceinline__ float dot2(unsigned k, unsigned q, float acc) {
  return __builtin_amdgcn_fdot2_f32_bf16(__builtin_bit_cast(bf16x2, k), __builtin_bit_cast(bf16x2, q), acc, false);
}
template <int CTRL> __device__ __forceinline__ float dpp(float x) {
  return __builtin_bit_cast(float, __builtin_amdgcn_mov_dpp(__builtin_bit_cast(int, x), CTRL, 0xf, 0xf, true));
}
constexpr int XOR1 = 0xB1, XOR2 = 0x4E, XOR7 = 0x141, XOR8 = 0x128, ROR4 = 0x124;

__device__ __forceinline__ float block_sum(float v, float* red) {
  for (int m = 32; m >= 1; m >>= 1) v += __shfl_xor(v, m);
  int w = threadIdx.x >> 6, nw = blockDim.x >> 6;
  __syncthreads();
  if ((threadIdx.x & 63) == 0) red[w] = v;
  __syncthreads();
  float s = 0; for (int i = 0; i < nw; ++i) s += red[i];
  return s;
}
__device__ void ln_row(const float* xs, float* xn, float* red) {
  float s = 0; for (int i = threadIdx.x; i < D; i += blockDim.x) s += xs[i];
  s = block_sum(s, red);
  float mean = s / D;
  float v = 0; for (int i = threadIdx.x; i < D; i += blockDim.x) { float d = xs[i] - mean; v += d * d; }
  v = block_sum(v, red);
  float var = v / D;
  float m = rm(mean), vv = rm(var);
  float den = rm(sqrtf(rm(vv + 1e-5f)));
  for (int i = threadIdx.x; i < D; i += blockDim.x) xn[i] = rbf(rm(xs[i] - m) / den);
  __syncthreads();
}

__global__ void __launch_bounds__(256) k_q(const bf16_t* x, const bf16_t* wq, bf16_t* q) {
  __shared__ float xs[D], xn[D], red[16];
  int b = blockIdx.y, n = blockIdx.x * 256 + threadIdx.x;
  for (int i = threadIdx.x; i < D; i += 256) xs[i] = bf2f(x[b * D + i]);
  __syncthreads();
  ln_row(xs, xn, red);
  float a = 0;
  for (int k = 0; k < D; ++k) a += xn[k] * bf2f(wq[(size_t)k * (H * DH) + n]);
  q[b * H * DH + n] = f2bf(a);
}

constexpr int NW = 8;
__global__ void __launch_bounds__(NW * 64) k_attn(const bf16_t* q, const bf16_t* kc, const bf16_t* vc, bf16_t* o) {
  __shared__ float mrg[NW * 4][DH + 4];
  const int bh = blockIdx.x;
  const int tid = threadIdx.x, lane = tid & 63, wave = __builtin_amdgcn_readfirstlane(tid >> 6), r = lane >> 4, c = lane & 15;
  const v4u qv = *reinterpret_cast<const v4u*>(q + bh * DH + 8 * c);
  const v4u* kp = reinterpret_cast<const v4u*>(kc + (size_t)bh * SK * DH) + lane;
  const v4u* vp = reinterpret_cast<const v4u*>(vc + (size_t)bh * SK * DH) + lane;
  constexpr int TL = 8;
  constexpr int NT = SK / (4 * TL) / NW;
  float m = -INFINITY, l = 0.f, acc[8];
#pragma unroll
  for (int k = 0; k < 8; ++k) acc[k] = 0.f;
  v4u Kn[TL], Vn[TL];
  {
    int t = wave;
#pragma unroll
    for (int i = 0; i < TL; ++i) Kn[i] = __builtin_nontemporal_load(kp + (t * TL + i) * 64);
#pragma unroll
    for (int i = 0; i < TL; ++i) Vn[i] = __builtin_nontemporal_load(vp + (t * TL + i) * 64);
  }
  for (int it = 0; it < NT; ++it) {
    v4u Kc[TL], Vc[TL];
#pragma unroll
    for (int i = 0; i < TL; ++i) { Kc[i] = Kn[i]; Vc[i] = Vn[i]; }
    if (it + 1 < NT) {
      int t = wave + (it + 1) * NW;
#pragma unroll
      for (int i = 0; i < TL; ++i) Kn[i] = __builtin_nontemporal_load(kp + (t * TL + i) * 64);
#pragma unroll
      for (int i = 0; i < TL; ++i) Vn[i] = __builtin_nontemporal_load(vp + (t * TL + i) * 64);
    }
    float x[TL];
#pragma unroll
    for (int i = 0; i < TL; ++i) { float d = 0.f;
#pragma unroll
      for (int k = 0; k < 4; ++k) d = dot2(Kc[i][k], qv[k], d);
      x[i] = d; }
#define TR_STEP(HALF, CTRL, BIT) _Pragma("unroll") for (int h = 0; h < (HALF); ++h) { \
      const float lo_ = x[h], hi_ = x[h + (HALF)]; const float t1_ = lo_ + dpp<CTRL>(lo_), t2_ = hi_ + dpp<CTRL>(hi_); \
      x[h] = (c & (BIT)) ? t2_ : t1_; }
    TR_STEP(4, XOR8, 8) TR_STEP(2, XOR7, 4) TR_STEP(1, XOR2, 2)
    float s = x[0] + dpp<XOR1>(x[0]);
    s = rm(rm(s) * 0.08838834764831845f);
    s *= 1.4426950408889634f;
    float pm = s; pm = fmaxf(pm, dpp<XOR2>(pm)); pm = fmaxf(pm, dpp<XOR7>(pm)); pm = fmaxf(pm, dpp<XOR8>(pm));
    const float mn = fmaxf(m, pm);
    const float alpha = __builtin_amdgcn_exp2f(m - mn), p = __builtin_amdgcn_exp2f(s - mn);
    l = fmaf(l, alpha, p); m = mn;
#pragma unroll
    for (int k = 0; k < 8; ++k) acc[k] *= alpha;
#pragma unroll
    for (int i = 0; i < TL; ++i) {
      float pi;
      switch (i) {
        case 0: pi = dpp<0x150>(p); break; case 1: pi = dpp<0x152>(p); break; case 2: pi = dpp<0x154>(p); break; case 3: pi = dpp<0x156>(p); break;
        case 4: pi = dpp<0x158>(p); break; case 5: pi = dpp<0x15A>(p); break; case 6: pi = dpp<0x15C>(p); break; default: pi = dpp<0x15E>(p); break; }
#pragma unroll
      for (int k = 0; k < 4; ++k) {
        acc[2 * k] = fmaf(pi, __builtin_bit_cast(float, Vc[i][k] << 16), acc[2 * k]);
        acc[2 * k + 1] = fmaf(pi, __builtin_bit_cast(float, Vc[i][k] & 0xffff0000u), acc[2 * k + 1]);
      }
    }
  }
  { float t = l; t += dpp<XOR2>(t); t += dpp<XOR7>(t); t += dpp<XOR8>(t); l = t; }
  float* rec = mrg[wave * 4 + r];
#pragma unroll
  for (int k = 0; k < 8; ++k) rec[4 + 8 * c + k] = acc[k];
  if (c == 0) { rec[0] = m; rec[1] = l; }
  __syncthreads();
  if (tid < DH) {
    float M = -INFINITY;
    for (int j = 0; j < NW * 4; ++j) M = fmaxf(M, mrg[j][0]);
    float L = 0.f, ov = 0.f;
    for (int j = 0; j < NW * 4; ++j) { float w = __builtin_amdgcn_exp2f(mrg[j][0] - M); L = fmaf(w, mrg[j][1], L); ov = fmaf(w, mrg[j][4 + tid], ov); }
    o[bh * DH + tid] = f2bf(ov / L);
  }
}

__global__ void __launch_bounds__(256) k_o(const bf16_t* x, const bf16_t* o, const bf16_t* wo, bf16_t* h) {
  __shared__ float red[4][64];
  int b = blockIdx.y, n = blockIdx.x * 64 + (threadIdx.x & 63), ks = threadIdx.x >> 6;
  float a = 0;
  for (int k = ks * 1024; k < ks * 1024 + 1024; ++k) a += bf2f(o[b * 4096 + k]) * bf2f(wo[(size_t)k * D + n]);
  red[ks][threadIdx.x & 63] = a;
  __syncthreads();
  if (ks == 0) { int j = threadIdx.x; float s = red[0][j] + red[1][j] + red[2][j] + red[3][j]; h[b * D + n] = f2bf(bf2f(x[b * D + n]) + rm(s)); }
}
__global__ void __launch_bounds__(256) k_gu(const bf16_t* h, const bf16_t* wg, const bf16_t* wu, bf16_t* act) {
  __shared__ float xs[D], xn[D], red[16];
  int b = blockIdx.y, n = blockIdx.x * 256 + threadIdx.x;
  for (int i = threadIdx.x; i < D; i += 256) xs[i] = bf2f(h[b * D + i]);
  __syncthreads();
  ln_row(xs, xn, red);
  float g = 0, u = 0;
  for (int k = 0; k < D; ++k) { g += xn[k] * bf2f(wg[(size_t)k * F + n]); u += xn[k] * bf2f(wu[(size_t)k * F + n]); }
  g = rm(g); u = rm(u);
  float inner = 0.7978845608028654f * (g + 0.044715f * g * g * g);
  float ge = rm(0.5f * g * (1.f + tanhf(inner)));
  act[b * F + n] = f2bf(ge * u);
}
__global__ void __launch_bounds__(256) k_d(const bf16_t* h, const bf16_t* act, const bf16_t* wd, bf16_t* out) {
  __shared__ float red[4][64];
  int b = blockIdx.y, n = blockIdx.x * 64 + (threadIdx.x & 63), ks = threadIdx.x >> 6;
  float a = 0;
  for (int k = ks * (F / 4); k < (ks + 1) * (F / 4); ++k) a += bf2f(act[b * F + k]) * bf2f(wd[(size_t)k * D + n]);
  red[ks][threadIdx.x & 63] = a;
  __syncthreads();
  if (ks == 0) { int j = threadIdx.x; float s = red[0][j] + red[1][j] + red[2][j] + red[3][j]; out[b * D + n] = f2bf(bf2f(h[b * D + n]) + rm(s)); }
}

extern "C" void kernel_launch(void* const* d_in, const int* in_sizes, int n_in, void* d_out, int out_size, void* d_ws, size_t ws_size, hipStream_t stream) {
  const bf16_t* x = (const bf16_t*)d_in[0];
  const bf16_t* kc = (const bf16_t*)d_in[1];
  const bf16_t* vc = (const bf16_t*)d_in[2];
  const bf16_t* wq = (const bf16_t*)d_in[3];
  const bf16_t* wo = (const bf16_t*)d_in[4];
  const bf16_t* wg = (const bf16_t*)d_in[6];
  const bf16_t* wu = (const bf16_t*)d_in[7];
  const bf16_t* wd = (const bf16_t*)d_in[8];
  bf16_t* ws = (bf16_t*)d_ws;
  bf16_t* q = ws; bf16_t* o = ws + 32768; bf16_t* h = ws + 65536; bf16_t* act = ws + 65536 + 8192;
  k_q<<<dim3(16, B), 256, 0, stream>>>(x, wq, q);
  k_attn<<<B * H, NW * 64, 0, stream>>>(q, kc, vc, o);
  k_o<<<dim3(16, B), 256, 0, stream>>>(x, o, wo, h);
  k_gu<<<dim3(14, B), 256, 0, stream>>>(h, wg, wu, act);
  k_d<<<dim3(16, B), 256, 0, stream>>>(h, act, wd, (bf16_t*)d_out);
}
```

```cpp
#include <hip/hip_runtime.h>
#include <stdint.h>

#define NFL 12
#define NTP 16
#define NTQ 16
#define ADAPT 1
#define LAGTHR 30
#define QDELAY 4
#define SLP_O 2
#define SLP_P 0
#define SLP_H 2
#define SLP_G 2
#define STAMPS 0

typedef unsigned short bf16_t;
typedef unsigned v4u __attribute__((ext_vector_type(4)));
typedef unsigned v2u __attribute__((ext_vector_type(2)));
typedef float f4 __attribute__((ext_vector_type(4)));
typedef float f2 __attribute__((ext_vector_type(2)));
typedef short v4s __attribute__((ext_vector_type(4)));
typedef short v8s __attribute__((ext_vector_type(8)));
typedef __bf16 bf16x2 __attribute__((ext_vector_type(2)));
typedef __bf16 bf16x8 __attribute__((ext_vector_type(8)));
typedef __amdgpu_buffer_rsrc_t rsrc_t;
#define LAS __attribute__((address_space(3)))
#define FENCE() asm volatile("" ::: "memory")

constexpr int D = 1024, NH = 32, SK = 4096, DH = 128, F = 3584;
constexpr int WS_EP = 0, WS_FL = 4096, WS_QX = 8192, WS_PX = WS_QX + 256 * 2048, WS_DX = WS_PX + 524288, WS_CLR = WS_DX + 262144;
constexpr int WS_OX = WS_CLR, WS_HX = WS_OX + 65536, WS_GX = WS_HX + 16384;
constexpr int WS_END = WS_GX + 2 * 57344, CLR_PER_WG = (WS_END - WS_CLR) / 256;
static_assert(CLR_PER_WG * 256 == WS_END - WS_CLR && CLR_PER_WG % 128 == 0 && CLR_PER_WG <= 1024, "clear shares");
constexpr int WS_ST = 4 << 20;
constexpr int RT = 9, RINGB = RT * 8192, MISC = 2 * RINGB;
constexpr int C_KL = MISC, C_VL = MISC + 4, C_PK = MISC + 16, C_PV = MISC + 32, C_WQ = MISC + 48, C_XN = MISC + 52, C_QR = MISC + 56,
              C_MG = MISC + 60, C_OS = MISC + 64, C_CD = MISC + 68, C_BD = MISC + 72, C_VF = MISC + 76, C_EP = MISC + 80, C_TS = MISC + 96, C_CT = MISC + 112;
constexpr int L_Q = MISC + 256, L_XN = MISC + 512, XNS = 528, L_MML = MISC + 512, L_MRG = MISC + 1024, L_OF = MISC + 9216, L_RED = MISC + 1024,
              L_CS = MISC + 9216, L_STAT = MISC + 13312;
constexpr int LDS_BYTES = MISC + 16384;
constexpr int AUX_NT = 2, AUX_SC1 = 16;
constexpr int OOB = 0x7fffff00;
constexpr unsigned SENT = 0xffffffffu, TAGM = 0xfu;
constexpr int SPIN_CAP = 1 << 20;
__host__ __device__ constexpr int pre(int X) { return X ? 3 : 1; }
__host__ __device__ constexpr int vtw(int X) { return 128 + pre(X); }
constexpr int CK_A = 16, CK_B = 40, CK_R = 52;

__device__ __forceinline__ float bflo(unsigned u) { return __builtin_bit_cast(float, u << 16); }
__device__ __forceinline__ float bfhi(unsigned u) { return __builtin_bit_cast(float, u & 0xffff0000u); }
__device__ __forceinline__ unsigned pk(float lo, float hi) { bf16x2 v = {(__bf16)lo, (__bf16)hi}; return __builtin_bit_cast(unsigned, v); }
__device__ __forceinline__ float rbf(float f) { return bflo(pk(f, f)); }
__device__ __forceinline__ unsigned nosent(unsigned u) { return u == SENT ? 0x7fc07fc0u : u; }
__device__ __forceinline__ float dot2(unsigned a, unsigned b, float c) {
  return __builtin_amdgcn_fdot2_f32_bf16(__builtin_bit_cast(bf16x2, a), __builtin_bit_cast(bf16x2, b), c, false);
}
template <int CTRL> __device__ __forceinline__ unsigned dppu(unsigned x) { return __builtin_amdgcn_mov_dpp(x, CTRL, 0xf, 0xf, true); }
template <int CTRL> __device__ __forceinline__ float dpp(float x) { return __builtin_bit_cast(float, dppu<CTRL>(__builtin_bit_cast(unsigned, x))); }
constexpr int XOR1 = 0xB1, XOR2 = 0x4E, XOR7 = 0x141, XOR8 = 0x128;
__device__ __forceinline__ float x16_sum(float x) {
  auto s = __builtin_amdgcn_permlane16_swap(__float_as_uint(x), __float_as_uint(x), false, false);
  return __uint_as_float(s[0]) + __uint_as_float(s[1]);
}
__device__ __forceinline__ float x32_sum(float x) {
  auto s = __builtin_amdgcn_permlane32_swap(__float_as_uint(x), __float_as_uint(x), false, false);
  return __uint_as_float(s[0]) + __uint_as_float(s[1]);
}
__device__ __forceinline__ float rows_sum(float x) { return x32_sum(x16_sum(x)); }
__device__ __forceinline__ float row16_sum(float x) { x += dpp<XOR1>(x); x += dpp<XOR2>(x); x += dpp<XOR7>(x); x += dpp<XOR8>(x); return x; }
__device__ __forceinline__ float row16_max(float x) { x = fmaxf(x, dpp<XOR1>(x)); x = fmaxf(x, dpp<XOR2>(x)); x = fmaxf(x, dpp<XOR7>(x)); return fmaxf(x, dpp<XOR8>(x)); }
__device__ __forceinline__ float wave_sum(float x) { return rows_sum(row16_sum(x)); }
__device__ __forceinline__ rsrc_t mkrs(const void* p, unsigned bytes) { return __builtin_amdgcn_make_buffer_rsrc((void*)p, 0, bytes, 0x00020000); }
__device__ __forceinline__ f4 mfma(v4u a, v4u b, f4 c) {
  return __builtin_amdgcn_mfma_f32_16x16x32_bf16(__builtin_bit_cast(bf16x8, a), __builtin_bit_cast(bf16x8, b), c, 0, 0, 0);
}
__device__ __forceinline__ unsigned now32() { return (unsigned)__builtin_amdgcn_s_memrealtime(); }
template <int N> __device__ __forceinline__ void waitvm() { asm volatile("s_waitcnt vmcnt(%0)" ::"n"(N) : "memory"); }
#define WG_BARRIER() asm volatile("s_waitcnt lgkmcnt(0)\n\ts_barrier" ::: "memory")
__device__ __forceinline__ void lds_put(int addr, unsigned v) {
  asm volatile("s_mov_b64 exec, 1\n\tds_write_b32 %0, %1\n\ts_mov_b64 exec, -1" ::"v"(addr), "v"(v) : "memory");
}
__device__ __forceinline__ void lds_max(int addr, unsigned v) {
  asm volatile("s_mov_b64 exec, 1\n\tds_max_u32 %0, %1\n\ts_mov_b64 exec, -1" ::"v"(addr), "v"(v) : "memory");
}
__device__ __forceinline__ unsigned lds_get(int addr) {
  unsigned v; asm volatile("ds_read_b32 %0, %1\n\ts_waitcnt lgkmcnt(0)" : "=v"(v) : "v"(addr) : "memory");
  return __builtin_amdgcn_readfirstlane(v);
}
__device__ __forceinline__ unsigned lds_inc(int addr) {
  unsigned o;
  asm volatile("s_waitcnt lgkmcnt(0)\n\ts_mov_b64 exec, 1\n\tds_add_rtn_u32 %0, %1, %2\n\ts_mov_b64 exec, -1\n\ts_waitcnt lgkmcnt(0)"
               : "=&v"(o) : "v"(addr), "v"(1u) : "memory");
  return __builtin_amdgcn_readfirstlane(o);
}
__device__ __forceinline__ void lds_add(int addr) {
  asm volatile("s_waitcnt lgkmcnt(0)\n\ts_mov_b64 exec, 1\n\tds_add_u32 %0, %1\n\ts_mov_b64 exec, -1" ::"v"(addr), "v"(1u) : "memory");
}
#define lds_wait_ge(addr, thr) do { while (lds_get(addr) < (unsigned)(thr) && --bud > 0) __builtin_amdgcn_s_sleep(1); } while (0)
#define lds_doze_ge(addr, thr) do { while (lds_get(addr) < (unsigned)(thr) && --bud > 0) __builtin_amdgcn_s_sleep(8); } while (0)
#define lds_spin_ge(addr, thr) do { while (lds_get(addr) < (unsigned)(thr) && --bud > 0) {} } while (0)
__device__ __forceinline__ v4u trfrag(LAS unsigned char* L, int a0, int a1) {
  v4s x = __builtin_amdgcn_ds_read_tr16_b64_v4i16((LAS v4s*)(L + a0));
  v4s y = __builtin_amdgcn_ds_read_tr16_b64_v4i16((LAS v4s*)(L + a1));
  return __builtin_bit_cast(v4u, (v8s)__builtin_shufflevector(x, y, 0, 1, 2, 3, 4, 5, 6, 7));
}
__device__ __forceinline__ bool has_sent(v4u v) { return max(max(v.x, v.y), max(v.z, v.w)) == SENT; }
__device__ __forceinline__ v4u mk_tag(v2u E) {
  const unsigned t0 = E.x & TAGM;
  return v4u{t0, ((E.x >> 4) + t0) & TAGM, ((E.x >> 8) + t0) & TAGM, ((E.x >> 12) + t0) & TAGM};
}
__device__ __forceinline__ v4u put_tag(f4 y, v4u T) { return (__builtin_bit_cast(v4u, y) & ~TAGM) | T; }
__device__ __forceinline__ bool bad_tag(v4u v, v4u T) { const v4u d = v ^ T; return ((d.x | d.y | d.z | d.w) & TAGM) != 0u; }
__device__ __forceinline__ f4 strip_tag(v4u v) { return __builtin_bit_cast(f4, v & ~TAGM); }
template <int N, int SLEEP, bool TAGGED, int AUX> __device__ __forceinline__ void gather(rsrc_t rs, const int (&off)[N], v4u (&v)[N], v4u T, int& bud) {
  bool bad[N];
#pragma unroll
  for (int j = 0; j < N; ++j) v[j] = __builtin_amdgcn_raw_buffer_load_b128(rs, off[j], 0, AUX);
  for (;;) {
    bool any = false;
#pragma unroll
    for (int j = 0; j < N; ++j) { bad[j] = TAGGED ? (bool)__any(bad_tag(v[j], T)) : has_sent(v[j]); any |= bad[j]; }
    if (!__any(any) || --bud <= 0) break;
    if (SLEEP) __builtin_amdgcn_s_sleep(SLEEP);
    FENCE();
    v4u t[N];
#pragma unroll
    for (int j = 0; j < N; ++j) t[j] = __builtin_amdgcn_raw_buffer_load_b128(rs, bad[j] ? off[j] : OOB, 0, AUX);
#pragma unroll
    for (int j = 0; j < N; ++j) v[j] = bad[j] ? t[j] : v[j];
  }
  FENCE();
}
__device__ __forceinline__ f4 comb2(bool lo, f4 a1A, f4 a2A, f4 a1B, f4 a2B) {
  f4 o;
#pragma unroll
  for (int i = 0; i < 4; ++i) { const float x = lo ? a1A[i] : a2B[i], y = lo ? a1B[i] : a2A[i]; o[i] = x + dpp<XOR8>(y); }
  return o;
}

struct Args {
  const bf16_t* x; const bf16_t* kc; const bf16_t* vc; const bf16_t* wq; const bf16_t* wo;
  const bf16_t* wg; const bf16_t* wu; const bf16_t* wd; bf16_t* out; unsigned char* ws;
};

#define STAMP(i) do {} while (0)
#define STAMP_OUT() do {} while (0)

__device__ __forceinline__ void dma(rsrc_t rs, LAS unsigned char* L, int lds, int voff, int soff) {
  __builtin_amdgcn_raw_ptr_buffer_load_lds(rs, (LAS void*)(L + lds), 16, voff, soff, 0, AUX_NT);
}

template <int N> __device__ __forceinline__ void wait_pub(int cl, int u, int j) {
  waitvm<N - 1>();
  if (((j - N + 2) & 3) == 0 && u - N + 2 > 0) lds_max(cl, u - N + 2);
}
template <int N> __device__ __forceinline__ void drain_pub(int cl, int n) {
  if constexpr (N > 13) { waitvm<12>(); lds_max(cl, n - 12); }
  if constexpr (N > 9) { waitvm<8>(); lds_max(cl, n - 8); }
  if constexpr (N > 5) { waitvm<4>(); lds_max(cl, n - 4); }
  waitvm<0>(); lds_max(cl, n);
}
template <int X> __device__ __forceinline__ int free_addr(int vt) { const int t = vt - RT - pre(X); return t < 0 ? C_WQ : (X ? C_PV : C_PK) + 4 * (t & 3); }
template <int X> __device__ __forceinline__ unsigned free_thr(int vt) { const int t = vt - RT - pre(X); return t < 0 ? 2u : (unsigned)(t + 1); }
template <int X> __device__ __forceinline__ void ring_free(int vt, unsigned have, int& bud) {
  if (have < free_thr<X>(vt)) {
    if (lds_get(free_addr<X>(vt)) < free_thr<X>(vt)) {
      waitvm<0>(); lds_max(X ? C_VL : C_KL, vt * 8);
      lds_wait_ge(free_addr<X>(vt), free_thr<X>(vt));
    }
  }
}
template <int N> __device__ __forceinline__ void kv_tile(rsrc_t rkv, LAS unsigned char* L, int lb, int lane, int so, int cl, int vt) {
#pragma unroll
  for (int j = 0; j < 8; ++j) { dma(rkv, L, lb + j * 1024, lane * 16, so + j * 1024); wait_pub<N>(cl, vt * 8 + j, j); }
}

template <int X> __device__ __forceinline__ void loader(const Args& a, LAS unsigned char* L, int lane, int blk) {
  const int b = blk >> 5, h = blk & 31, row = blk >> 3;
  constexpr int cl = X ? C_VL : C_KL, ring = X * RINGB, VW = vtw(X), UW = VW * 8;
  int bud = SPIN_CAP;
  unsigned tv = 0;
  const int v128 = (lane >> 3), sw128 = ((lane & 7) ^ (((lane >> 4) & 3) << 1)) * 16;
  const rsrc_t rkv = mkrs((X ? a.vc : a.kc) + (size_t)blk * SK * DH, SK * DH * 2);
  {
    const rsrc_t rs = mkrs(a.wq, D * NH * DH * 2);
    const int voff = v128 * 8192 + sw128, s0 = (256 * (b >> 1) + 64 * X) * 8192 + (h * 128 + 64 * (b & 1)) * 2;
#pragma unroll
    for (int u = 0; u < 8 * pre(X); ++u) {
      dma(rs, L, ring + u * 1024, voff, s0 + u * 65536);
      if (u == 7) { asm volatile("s_barrier" ::: "memory"); }
      if (u >= NFL - 1) wait_pub<NFL>(cl, u, u & 7);
    }
  }
  int st = pre(X);
  int nsel = 1;
  unsigned t_ref = 0, tau = 0;
  for (int vt = pre(X); vt < VW; ++vt) {
    unsigned have; v2u ct; unsigned long long t64;
    asm volatile("ds_read_b32 %0, %3\n\tds_read_b64 %1, %4\n\ts_memrealtime %2" : "=&v"(have), "=&v"(ct), "=&s"(t64) : "v"(free_addr<X>(vt + 1)), "v"(C_CT) : "memory");
    const int lb = ring + st * 8192, so = (vt - pre(X)) * 8192;
    if (!ADAPT || nsel == 1) kv_tile<NFL>(rkv, L, lb, lane, so, cl, vt);
    else if (nsel == 0) kv_tile<NFL - 1>(rkv, L, lb, lane, so, cl, vt);
    else kv_tile<NFL + 1>(rkv, L, lb, lane, so, cl, vt);
    asm volatile("s_waitcnt lgkmcnt(0)" : "+v"(have), "+v"(ct), "+s"(t64)::"memory");
    const unsigned tn = (unsigned)t64;
    if (X == 0 && (vt == CK_A || vt == CK_B)) lds_put(C_TS + (vt == CK_B ? 4 : 0), tn | 1u);
    if (ADAPT) {
      t_ref = __builtin_amdgcn_readfirstlane(ct.x); tau = __builtin_amdgcn_readfirstlane(ct.y);
      const int lag = (int)(tn - t_ref) - (int)(((unsigned)(vt - CK_B) * tau) >> 8);
      nsel = tau == 0u ? 1 : lag > LAGTHR ? 2 : lag < -LAGTHR ? 0 : 1;
    }
    if (vt + 1 >= RT) ring_free<X>(vt + 1, __builtin_amdgcn_readfirstlane(have), bud);
    st = st == RT - 1 ? 0 : st + 1;
  }
  STAMP(8 + X);
  if (row < 14) {
    const int pi = row * 8 + (blk & 7);
    const rsrc_t rs = mkrs((pi & 1) ? a.wu : a.wg, D * F * 2);
    const int voff = v128 * 7168 + sw128, s0 = 512 * X * 7168 + (pi >> 1) * 128;
#pragma unroll
    for (int v = 0; v < 8; ++v) {
      if (v) ring_free<X>(VW + v, 0u, bud);
#pragma unroll
      for (int j = 0; j < 8; ++j) {
        const int q = v * 8 + j;
        dma(rs, L, ring + ((VW + v) % RT) * 8192 + j * 1024, voff, s0 + q * 57344);
        if (q < NFL) wait_pub<NFL>(cl, UW + q, j); else if (q >= NTP) wait_pub<NTP>(cl, UW + q, j);
      }
    }
    drain_pub<NTP>(cl, UW + 64);
  } else if (row < 30) {
    const int qi = (row - 14) * 8 + (blk & 7);
    {
      const rsrc_t rs = mkrs(a.wo, NH * DH * D * 2);
      const int r4 = lane >> 4, pos = lane & 15, s0 = (256 * (qi >> 3) + 128 * X) * 2048 + (qi & 7) * 256;
      const int voff0 = r4 * 2048 + (pos ^ (r4 << 1)) * 16, voff1 = r4 * 2048 + (pos ^ ((r4 | 4) << 1)) * 16;
#pragma unroll
      for (int v = 0; v < 4; ++v) {
        if (v) ring_free<X>(VW + v, 0u, bud);
#pragma unroll
        for (int j = 0; j < 8; ++j) {
          const int q = v * 8 + j;
          dma(rs, L, ring + ((VW + v) % RT) * 8192 + j * 1024, (j & 2) ? voff1 : voff0, s0 + q * 8192);
          if (q < NFL) wait_pub<NFL>(cl, UW + q, j); else if (q >= NTQ) wait_pub<NTQ>(cl, UW + q, j);
        }
      }
      drain_pub<NTQ>(cl, UW + 32);
    }
    STAMP(28 + X);
    lds_wait_ge(C_CD, 1);
    {
      const rsrc_t rs = mkrs(a.wd, F * D * 2);
      const int voff = v128 * 2048 + sw128, s0 = (448 * (qi >> 4) + 256 * X) * 2048 + (qi & 15) * 128;
#pragma unroll
      for (int q = 0; q < (X ? 24 : 32); ++q) dma(rs, L, ring + ((VW + 4 + (q >> 3)) % RT) * 8192 + (q & 7) * 1024, voff, s0 + q * 16384);
      waitvm<X ? 8 : 16>(); lds_max(cl, UW + 48); waitvm<0>(); lds_max(cl, UW + 64);
    }
  } else {
    drain_pub<NFL + 1>(cl, UW);
  }
  STAMP(10 + X);
  STAMP_OUT();
  asm volatile("s_waitcnt vmcnt(0) lgkmcnt(0)" ::: "memory");
}

__device__ __forceinline__ void ln_params(float s, float ss, float& m, float& inv) {
  const float mean = s * (1.f / D), var = fmaxf(fmaf(-mean, mean, ss * (1.f / D)), 0.f);
  m = rbf(mean);
  inv = __builtin_amdgcn_rcpf(rbf(__builtin_amdgcn_sqrtf(rbf(rbf(var) + 1e-5f))));
}
__device__ __forceinline__ unsigned ln_pair(unsigned u, float m, float inv) { const unsigned d = pk(bflo(u) - m, bfhi(u) - m); return pk(bflo(d) * inv, bfhi(d) * inv); }
__device__ __forceinline__ float sum8(v4u u, float s) { return dot2(u.w, 0x3f803f80u, dot2(u.z, 0x3f803f80u, dot2(u.y, 0x3f803f80u, dot2(u.x, 0x3f803f80u, s)))); }
__device__ __forceinline__ float sq8(v4u u, float s) { return dot2(u.w, u.w, dot2(u.z, u.z, dot2(u.y, u.y, dot2(u.x, u.x, s)))); }

__device__ __forceinline__ void service(const Args& a, LAS unsigned char* L, int lane, int blk) {
  const int row = blk >> 3, g = lane >> 4, m = lane & 15;
  const rsrc_t ws = mkrs(a.ws, WS_END);
  int bud = SPIN_CAP;
  unsigned tv = 0;
  const v2u ep = __builtin_amdgcn_raw_buffer_load_b64(ws, WS_EP, 0, AUX_SC1);
  ((LAS unsigned*)(L + MISC))[lane] = 0u;
  WG_BARRIER();
  const unsigned long long e = (((unsigned long long)ep.y << 32) | ep.x) + 1ull;
  const v2u E = {(unsigned)e, (unsigned)(e >> 32)};
  if (lane == 0) *(LAS v2u*)(L + C_EP) = E;
  FENCE();
  lds_add(C_XN);
  lds_wait_ge(C_WQ, 2);
  __builtin_amdgcn_raw_buffer_store_b128(v4u{SENT, SENT, SENT, SENT}, ws, lane * 16 < CLR_PER_WG ? WS_CLR + blk * CLR_PER_WG + lane * 16 : OOB, 0, AUX_SC1 | AUX_NT);
  lds_doze_ge(C_TS + 4, 1);
  waitvm<0>();
  {
    const unsigned ta = lds_get(C_TS), tb = lds_get(C_TS + 4);
    __builtin_amdgcn_raw_buffer_store_b128(v4u{E.x, E.y, ta, tb}, ws, lane == 0 ? WS_FL + blk * 16 : OOB, 0, AUX_SC1);
  }
  lds_doze_ge(C_KL, 8 * CK_R);
  {
    v4u f[4];
    for (;;) {
      FENCE();
      bool ok = true;
#pragma unroll
      for (int j = 0; j < 4; ++j) f[j] = __builtin_amdgcn_raw_buffer_load_b128(ws, WS_FL + j * 1024 + lane * 16, 0, AUX_SC1);
#pragma unroll
      for (int j = 0; j < 4; ++j) ok &= (f[j].x == E.x) & (f[j].y == E.y);
      if (__all(ok) || --bud <= 0) break;
      __builtin_amdgcn_s_sleep(32);
    }
    if (blk == 0) __builtin_amdgcn_raw_buffer_store_b64(E, ws, lane == 0 ? WS_EP : OOB, 0, AUX_SC1);
    const unsigned t0 = __builtin_amdgcn_readfirstlane(f[0].w);
    float sb = 0.f, sd = 0.f;
#pragma unroll
    for (int j = 0; j < 4; ++j) { sb += (float)(int)(f[j].w - t0); sd += (float)(int)(f[j].w - f[j].z); }
    sb = wave_sum(sb) * (1.f / 256); sd = wave_sum(sd) * (1.f / (CK_B - CK_A));
    if (lane == 0) *(LAS v2u*)(L + C_CT) = v2u{t0 + (unsigned)(int)sb, (unsigned)sd};
    FENCE();
    lds_put(C_VF, 1);
  }
  if (row < 14 || row >= 30) return;
  const int qi = (row - 14) * 8 + (blk & 7), Tg = qi >> 2, gR = qi & 3;
  const v4u T = mk_tag(E);
  const int xo = ((m >> 1) * D + 32 * Tg + 16 * (m & 1) + 4 * gR) * 2;
  const v2u xres = __builtin_amdgcn_raw_buffer_load_b64(mkrs(a.x, 8 * D * 2), xo, 0, 0);
  float hres[4];
  lds_doze_ge(C_KL, 8 * 120);
  lds_wait_ge(C_BD, 4);
  {
    int off[4]; v4u pv[4];
#pragma unroll
    for (int i = 0; i < 4; ++i) off[i] = WS_PX + qi * 4096 + i * 1024 + lane * 16;
    gather<4, SLP_P, true, AUX_SC1>(ws, off, pv, T, bud);
    STAMP(12);
    const f4 s = (strip_tag(pv[0]) + strip_tag(pv[1])) + (strip_tag(pv[2]) + strip_tag(pv[3]));
    const float xs[4] = {bflo(xres.x), bfhi(xres.x), bflo(xres.y), bfhi(xres.y)};
#pragma unroll
    for (int i = 0; i < 4; ++i) hres[i] = xs[i] + rbf(rows_sum(s[i]));
    const v2u hp = {pk(hres[0], hres[1]), pk(hres[2], hres[3])};
    __builtin_amdgcn_raw_buffer_store_b64(v2u{nosent(hp.x), nosent(hp.y)}, ws,
        g == 0 ? WS_HX + (Tg >> 1) * 1024 + (16 * gR + 8 * (Tg & 1)) * 16 + m * 8 : OOB, 0, AUX_SC1);
    lds_put(C_CD, 1);
    hres[0] = bflo(hp.x); hres[1] = bfhi(hp.x); hres[2] = bflo(hp.y); hres[3] = bfhi(hp.y);
    STAMP(13);
  }
  WG_BARRIER();
  {
    int off[2]; v4u pv[2];
#pragma unroll
    for (int i = 0; i < 2; ++i) off[i] = WS_DX + qi * 2048 + i * 1024 + lane * 16;
    __builtin_amdgcn_s_sleep(8);
    gather<2, 0, true, AUX_SC1>(ws, off, pv, T, bud);
    STAMP(17);
    const f4 s = strip_tag(pv[0]) + strip_tag(pv[1]);
    float o[4];
#pragma unroll
    for (int i = 0; i < 4; ++i) o[i] = hres[i] + rbf(rows_sum(s[i]));
    __builtin_amdgcn_raw_buffer_store_b64(v2u{pk(o[0], o[1]), pk(o[2], o[3])}, mkrs(a.out, 8 * D * 2), g == 0 ? xo : OOB, 0, 0);
    STAMP(18);
  }
  STAMP_OUT();
}

__global__ void __launch_bounds__(448, 2) fused_decode(Args a) {
  extern __shared__ __attribute__((aligned(16))) unsigned char smem[];
  LAS unsigned char* L = (LAS unsigned char*)smem;
  const int tid = threadIdx.x, lane = tid & 63, wave = __builtin_amdgcn_readfirstlane(tid >> 6), blk = blockIdx.x;
  if (wave == 0) { __builtin_amdgcn_s_setprio(3); loader<0>(a, L, lane, blk); return; }
  if (wave == 1) { __builtin_amdgcn_s_setprio(3); loader<1>(a, L, lane, blk); return; }
  if (wave == 6) { service(a, L, lane, blk); return; }

  const int w = wave - 2, b = blk >> 5, h = blk & 31, row = blk >> 3;
  const int g = lane >> 4, m = lane & 15, q4 = (lane >> 2) & 3, p4 = lane & 3;
  const bool lo = m < 8;
  int bud = SPIN_CAP;
  unsigned tv = 0;
  const rsrc_t ws = mkrs(a.ws, WS_END);
  const int r8 = 4 * (g & 1) + q4;
  const int ta0 = (g >> 1) * 1024 + r8 * 128 + 8 * p4;
  if (w == 0) STAMP(0);
  v4u T;
  {
    const rsrc_t rx = mkrs(a.x, 8 * D * 2);
    v4u xr[2][2];
#pragma unroll
    for (int i = 0; i < 2; ++i)
#pragma unroll
      for (int j = 0; j < 2; ++j) xr[i][j] = __builtin_amdgcn_raw_buffer_load_b128(rx, (2 * w + i) * 2048 + j * 1024 + lane * 16, 0, 0);
    WG_BARRIER();
    const int kq = b >> 1;
#pragma unroll
    for (int i = 0; i < 2; ++i) {
      float mu, inv;
      ln_params(wave_sum(sum8(xr[i][1], sum8(xr[i][0], 0.f))), wave_sum(sq8(xr[i][1], sq8(xr[i][0], 0.f))), mu, inv);
      const v4u u = (kq & 2) ? xr[i][1] : xr[i][0];
      const v4u y = {ln_pair(u.x, mu, inv), ln_pair(u.y, mu, inv), ln_pair(u.z, mu, inv), ln_pair(u.w, mu, inv)};
      if ((lane >> 5) == (kq & 1)) *(LAS v4u*)(L + L_XN + (2 * w + i) * XNS + (lane & 31) * 16) = y;
    }
    FENCE();
    lds_add(C_XN);
    lds_spin_ge(C_XN, 5);
    T = mk_tag(*(LAS v2u*)(L + C_EP));
    if (w == 0) STAMP(20);
    if (w < 2) {
      v4u xb[4];
#pragma unroll
      for (int s = 0; s < 4; ++s) {
        const int o = L_XN + (m & 7) * XNS + (32 * (2 * s + (m >> 3)) + 4 * g) * 2;
        const v2u u0 = *(LAS v2u*)(L + o), u1 = *(LAS v2u*)(L + o + 32);
        xb[s] = v4u{u0.x, u0.y, u1.x, u1.y};
      }
      f4 a1[2], a2[2];
      a1[0] = a1[1] = a2[0] = a2[1] = f4{0.f, 0.f, 0.f, 0.f};
      lds_spin_ge(C_KL, 8); lds_spin_ge(C_VL, 24);
      if (w == 0) STAMP(1);
#pragma unroll
      for (int s = 0; s < 4; ++s)
#pragma unroll
        for (int t = 0; t < 2; ++t) {
          const int base = (s ? RINGB + (s - 1) * 8192 : 0) + ta0 + (((2 * w + t) ^ (r8 >> 1)) * 32);
          a1[t] = mfma(trfrag(L, base, base + 2048), xb[s], a1[t]);
          a2[t] = mfma(trfrag(L, base + 4096, base + 6144), xb[s], a2[t]);
        }
      __builtin_amdgcn_raw_buffer_store_b128(put_tag(comb2(lo, a1[0], a2[0], a1[1], a2[1]), T), ws,
          WS_QX + ((h * 8 + (m & 7)) * 4 + kq) * 512 + ((b & 1) * 16 + (2 * w + (m >> 3)) * 4 + g) * 16, 0, AUX_SC1 | AUX_NT);
      FENCE();
      lds_add(C_WQ);
      if (w == 0) STAMP(21);
    } else if (w == 2) {
      lds_wait_ge(C_WQ, 2);
      for (int i = 0; i < QDELAY; ++i) __builtin_amdgcn_s_sleep(8);
      int off[2]; v4u t[2];
#pragma unroll
      for (int j = 0; j < 2; ++j) off[j] = WS_QX + (h * 8 + b) * 2048 + j * 1024 + lane * 16;
      gather<2, 4, true, AUX_SC1 | AUX_NT>(ws, off, t, T, bud);
      const f4 s = strip_tag(t[0]) + strip_tag(t[1]);
      const v2u qp = {pk(x32_sum(s[0]), x32_sum(s[1])), pk(x32_sum(s[2]), x32_sum(s[3]))};
      if (lane < 32) *(LAS v2u*)(L + L_Q + lane * 8) = qp;
      FENCE();
      lds_put(C_QR, 1);
      STAMP(2);
    }
    lds_wait_ge(C_QR, 1);
  }
  {
    float acc[8], mx = -INFINITY, l = 0.f;
    const v4u qv = *(LAS v4u*)(L + L_Q + m * 16);
#pragma unroll
    for (int k = 0; k < 8; ++k) acc[k] = 0.f;
    int sk = w + pre(0), sv = w + pre(1);
    for (int it = 0; it < 32; ++it) {
      const int t = 4 * it + w;
      lds_wait_ge(C_KL, 8 * (t + pre(0) + 1));
      const int ok = sk * 8192 + lane * 16, ov = RINGB + sv * 8192 + lane * 16;
      float x[8];
      {
        v4u k[8];
#pragma unroll
        for (int i = 0; i < 8; ++i) k[i] = *(LAS v4u*)(L + ok + i * 1024);
#pragma unroll
        for (int i = 0; i < 8; ++i) x[i] = dot2(k[i].w, qv.w, dot2(k[i].z, qv.z, dot2(k[i].y, qv.y, dot2(k[i].x, qv.x, 0.f))));
      }
#define TR_STEP(HALF, CTRL, BIT) _Pragma("unroll") for (int hh = 0; hh < (HALF); ++hh) { \
        const float lo_ = x[hh], hi_ = x[hh + (HALF)]; const bool up_ = m & (BIT); \
        x[hh] = (up_ ? hi_ : lo_) + dpp<CTRL>(up_ ? lo_ : hi_); }
      TR_STEP(4, XOR8, 8) TR_STEP(2, XOR7, 4) TR_STEP(1, XOR2, 2)
#undef TR_STEP
      float s = x[0] + dpp<XOR1>(x[0]);
      asm volatile("" : "+v"(s));
      lds_put(C_PK + 4 * w, t + 1);
      s = rbf(rbf(s) * 0.08838834764831845f) * 1.4426950408889634f;
      float pm = fmaxf(s, dpp<XOR2>(s)); pm = fmaxf(pm, dpp<XOR7>(pm)); pm = fmaxf(pm, dpp<XOR8>(pm));
      const float mn = fmaxf(mx, pm);
      const float alpha = __builtin_amdgcn_exp2f(mx - mn), p = __builtin_amdgcn_exp2f(s - mn);
      l = fmaf(l, alpha, p); mx = mn;
      unsigned pp = pk(p, p), plo = pp & 0xffffu, phi = pp & 0xffff0000u;
#pragma unroll
      for (int k = 0; k < 8; ++k) acc[k] *= alpha;
#define PIN_ACC() asm volatile("" : "+v"(acc[0]), "+v"(acc[1]), "+v"(acc[2]), "+v"(acc[3]), "+v"(acc[4]), "+v"(acc[5]), "+v"(acc[6]), "+v"(acc[7]), "+v"(plo), "+v"(phi))
#define PV4(i0) { v4u v[4]; _Pragma("unroll") for (int i = 0; i < 4; ++i) v[i] = *(LAS v4u*)(L + ov + ((i0) + i) * 1024); \
        const unsigned a_[4] = {dppu<0x150 + 2 * (i0)>(plo), dppu<0x152 + 2 * (i0)>(plo), dppu<0x154 + 2 * (i0)>(plo), dppu<0x156 + 2 * (i0)>(plo)}; \
        const unsigned b_[4] = {dppu<0x150 + 2 * (i0)>(phi), dppu<0x152 + 2 * (i0)>(phi), dppu<0x154 + 2 * (i0)>(phi), dppu<0x156 + 2 * (i0)>(phi)}; \
        _Pragma("unroll") for (int i = 0; i < 4; ++i) _Pragma("unroll") for (int k = 0; k < 4; ++k) { \
          acc[2 * k] = dot2(v[i][k], a_[i], acc[2 * k]); acc[2 * k + 1] = dot2(v[i][k], b_[i], acc[2 * k + 1]); } }
      PIN_ACC();
      lds_wait_ge(C_VL, 8 * (t + pre(1)) + 4);
      PV4(0)
      PIN_ACC();
      lds_wait_ge(C_VL, 8 * (t + pre(1)) + 8);
      PV4(4)
      PIN_ACC();
#undef PV4
      lds_put(C_PV + 4 * w, t + 1);
      sk = sk >= RT - 4 ? sk - (RT - 4) : sk + 4;
      sv = sv >= RT - 4 ? sv - (RT - 4) : sv + 4;
    }
    if (w == 3) STAMP(3);
    l += dpp<XOR2>(l); l += dpp<XOR7>(l); l += dpp<XOR8>(l);
    *(LAS f4*)(L + L_MRG + (w * 4 + g) * 512 + m * 32) = f4{acc[0], acc[1], acc[2], acc[3]};
    *(LAS f4*)(L + L_MRG + (w * 4 + g) * 512 + m * 32 + 16) = f4{acc[4], acc[5], acc[6], acc[7]};
    if (m == 0) *(LAS f2*)(L + L_MML + (w * 4 + g) * 8) = f2{mx, l};
    FENCE();
    if (lds_inc(C_MG) == 3) {
      const f2 ml = *(LAS f2*)(L + L_MML + m * 8);
      f2 r[16];
#pragma unroll
      for (int j = 0; j < 16; ++j) r[j] = *(LAS f2*)(L + L_MRG + j * 512 + lane * 8);
      float e = __builtin_amdgcn_exp2f(ml.x - row16_max(ml.x));
      e *= __builtin_amdgcn_rcpf(row16_sum(ml.y * e));
      float o0 = 0.f, o1 = 0.f, o2 = 0.f, o3 = 0.f;
#define MG(j) { const float e0 = dpp<0x150 + (j)>(e), e1 = dpp<0x151 + (j)>(e); o0 = fmaf(e0, r[j].x, o0); o1 = fmaf(e0, r[j].y, o1); o2 = fmaf(e1, r[j + 1].x, o2); o3 = fmaf(e1, r[j + 1].y, o3); }
      MG(0) MG(2) MG(4) MG(6) MG(8) MG(10) MG(12) MG(14)
#undef MG
      lds_spin_ge(C_VF, 1);
      __builtin_amdgcn_raw_buffer_store_b32(nosent(pk(o0 + o2, o1 + o3)), ws, WS_OX + ((h >> 1) * 16 + (h & 1) * 8 + b) * 256 + lane * 4, 0, AUX_SC1);
      STAMP(4);
    }
  }
  if (row < 14) {
    const int pi = row * 8 + (blk & 7), X = w >> 1;
    v4u wf[8][4];
    f4 a1[4], a2[4];
#pragma unroll
    for (int t = 0; t < 4; ++t) a1[t] = f4{0.f, 0.f, 0.f, 0.f};
#pragma unroll
    for (int v = 0; v < 4; ++v) {
      lds_wait_ge(X ? C_VL : C_KL, 8 * ((X ? vtw(1) : vtw(0)) + 4 * (w & 1) + v + 1));
      const int s0 = X ? ((w & 1) ? (vtw(1) + 4 + v) % RT : (vtw(1) + v) % RT) : ((w & 1) ? (vtw(0) + 4 + v) % RT : (vtw(0) + v) % RT);
      const int sb = X * RINGB + s0 * 8192;
#pragma unroll
      for (int hs = 0; hs < 2; ++hs)
#pragma unroll
        for (int t = 0; t < 4; ++t) {
          const int ad = sb + hs * 4096 + ta0 + ((t ^ (r8 >> 1)) * 32);
          wf[2 * v + hs][t] = trfrag(L, ad, ad + 2048);
          a1[t] = mfma(wf[2 * v + hs][t], v4u{0x3f803f80u, 0x3f803f80u, 0x3f803f80u, 0x3f803f80u}, a1[t]);
        }
    }
    *(LAS f4*)(L + L_CS + w * 512 + (g * 2 + (m >> 3)) * 64 + (m & 1) * 16) = (m & 1) ? (lo ? a1[2] : a1[3]) : (lo ? a1[0] : a1[1]);
    if (w == 0) STAMP(5);
    int off[4]; v4u hv[4];
#pragma unroll
    for (int s = 0; s < 4; ++s) off[s] = WS_HX + (4 * w + s) * 1024 + lane * 16;
    lds_spin_ge(C_VF, 1);
    gather<4, SLP_H, false, AUX_SC1>(ws, off, hv, T, bud);
    if (w == 0) STAMP(6);
#pragma unroll
    for (int t = 0; t < 4; ++t) a1[t] = a2[t] = f4{0.f, 0.f, 0.f, 0.f};
#pragma unroll
    for (int s = 0; s < 4; ++s)
#pragma unroll
      for (int t = 0; t < 4; ++t) { a1[t] = mfma(wf[2 * s][t], hv[s], a1[t]); a2[t] = mfma(wf[2 * s + 1][t], hv[s], a2[t]); }
    float sm = sum8(hv[1], sum8(hv[0], 0.f)) + sum8(hv[3], sum8(hv[2], 0.f));
    float s2 = sq8(hv[1], sq8(hv[0], 0.f)) + sq8(hv[3], sq8(hv[2], 0.f));
    sm += dpp<XOR8>(sm); s2 += dpp<XOR8>(s2);
    if (lo) *(LAS f2*)(L + L_STAT + ((m & 7) * 16 + w * 4 + g) * 8) = f2{sm, s2};
    *(LAS f4*)(L + L_RED + (w * 2 + 0) * 1024 + lane * 16) = comb2(lo, a1[0], a2[0], a1[1], a2[1]);
    *(LAS f4*)(L + L_RED + (w * 2 + 1) * 1024 + lane * 16) = comb2(lo, a1[2], a2[2], a1[3], a2[3]);
    WG_BARRIER();
    if (w < 2) {
      f4 r[4], c[4], sp[8];
#pragma unroll
      for (int j = 0; j < 8; ++j) sp[j] = *(LAS f4*)(L + L_STAT + (m & 7) * 128 + j * 16);
#pragma unroll
      for (int j = 0; j < 4; ++j) { r[j] = *(LAS f4*)(L + L_RED + (j * 2 + w) * 1024 + lane * 16); c[j] = *(LAS f4*)(L + L_CS + j * 512 + (g * 2 + (m >> 3)) * 64 + w * 16); }
      const f4 sq = ((sp[0] + sp[1]) + (sp[2] + sp[3])) + ((sp[4] + sp[5]) + (sp[6] + sp[7]));
      float mu, inv; ln_params(sq[0] + sq[2], sq[1] + sq[3], mu, inv);
      f4 y = (((r[0] + r[1]) + (r[2] + r[3])) - mu * ((c[0] + c[1]) + (c[2] + c[3]))) * inv;
      if (!(pi & 1)) {
#pragma unroll
        for (int i = 0; i < 4; ++i) {
          const float gg = rbf(y[i]);
          const float z = gg * fmaf(0.044715f * gg, gg, 1.f);
          y[i] = gg * __builtin_amdgcn_rcpf(1.f + __builtin_amdgcn_exp2f(-2.3022081982f * z));
        }
      }
      __builtin_amdgcn_raw_buffer_store_b64(v2u{nosent(pk(y[0], y[1])), nosent(pk(y[2], y[3]))}, ws,
          WS_GX + (pi & 1) * 57344 + (pi >> 1) * 1024 + (16 * g + 8 * w + (m & 7)) * 16 + (m >> 3) * 8, 0, AUX_SC1);
      if (w == 0) STAMP(7);
    }
  } else if (row < 30) {
    const int qi = (row - 14) * 8 + (blk & 7);
    {
      v4u wf[8][2];
      const int tb = 2 * g * 1024 + q4 * 256 + 8 * p4, fx = q4 | ((g & 1) << 2);
#pragma unroll
      for (int Tk = 0; Tk < 8; ++Tk) {
        lds_wait_ge(Tk >= 4 ? C_VL : C_KL, 8 * (vtw(Tk >> 2) + (Tk & 3) + 1));
#pragma unroll
        for (int t = 0; t < 2; ++t) { const int ad = (Tk >> 2) * RINGB + ((vtw(Tk >> 2) + (Tk & 3)) % RT) * 8192 + tb + (((2 * w + t) ^ fx) * 32); wf[Tk][t] = trfrag(L, ad, ad + 1024); }
      }
      if (w == 0) STAMP(5);
      int off[1] = {WS_OX + ((qi >> 3) * 16 + 4 * w + g) * 256 + m * 16};
      v4u ov[1];
      lds_spin_ge(C_VF, 1);
      gather<1, SLP_O, false, AUX_SC1>(ws, off, ov, T, bud);
      const int Tk = 4 * (w >> 1) + (m >> 2);
      *(LAS v4u*)(L + L_OF + (Tk >> 1) * 1024 + (16 * (m & 3) + 8 * (Tk & 1) + ((4 * w + g) & 7)) * 16) = ov[0];
      FENCE();
      lds_add(C_OS);
      lds_spin_ge(C_OS, 4);
      if (w == 0) STAMP(6);
      f4 a1[2], a2[2];
      a1[0] = a1[1] = a2[0] = a2[1] = f4{0.f, 0.f, 0.f, 0.f};
#pragma unroll
      for (int s = 0; s < 4; ++s) {
        const v4u y = *(LAS v4u*)(L + L_OF + s * 1024 + lane * 16);
#pragma unroll
        for (int t = 0; t < 2; ++t) { a1[t] = mfma(wf[2 * s][t], y, a1[t]); a2[t] = mfma(wf[2 * s + 1][t], y, a2[t]); }
      }
      __builtin_amdgcn_raw_buffer_store_b128(put_tag(comb2(lo, a1[0], a2[0], a1[1], a2[1]), T), ws,
          WS_PX + (((4 * (qi & 7) + w) * 4 + g) * 16 + (qi >> 3)) * 256 + (m & 7) * 32 + (m >> 3) * 16, 0, AUX_SC1);
      lds_add(C_BD);
      if (w == 0) STAMP(7);
    }
    {
      const int ks = qi >> 4, cj = qi & 15, X = w >> 1;
      v4u wf[4][4];
      lds_wait_ge(X ? C_VL : C_KL, 8 * (X ? vtw(1) : vtw(0)) + 48 + 16 * (w & 1));
#pragma unroll
      for (int v = 0; v < 2; ++v) {
        const int s0 = X ? ((w & 1) ? (vtw(1) + 6 + v) % RT : (vtw(1) + 4 + v) % RT) : ((w & 1) ? (vtw(0) + 6 + v) % RT : (vtw(0) + 4 + v) % RT);
        const int sb = X * RINGB + s0 * 8192;
#pragma unroll
        for (int hs = 0; hs < 2; ++hs)
#pragma unroll
          for (int t = 0; t < 4; ++t) { const int ad = sb + hs * 4096 + ta0 + ((t ^ (r8 >> 1)) * 32); wf[2 * v + hs][t] = trfrag(L, ad, ad + 2048); }
      }
      if (w == 0) STAMP(14);
      int off[4]; v4u gu[4];
#pragma unroll
      for (int v = 0; v < 2; ++v) {
        const bool live = v == 0 || w < 3;
        off[2 * v] = live ? WS_GX + (7 * ks + 2 * w + v) * 1024 + lane * 16 : OOB;
        off[2 * v + 1] = live ? off[2 * v] + 57344 : OOB;
      }
      gather<4, SLP_G, false, AUX_SC1>(ws, off, gu, T, bud);
      if (w == 0) STAMP(15);
      f4 a1[4], a2[4];
#pragma unroll
      for (int t = 0; t < 4; ++t) a1[t] = a2[t] = f4{0.f, 0.f, 0.f, 0.f};
#pragma unroll
      for (int v = 0; v < 2; ++v) {
        if (v == 0 || w < 3) {
          v4u y;
#pragma unroll
          for (int k = 0; k < 4; ++k) y[k] = pk(bflo(gu[2 * v][k]) * bflo(gu[2 * v + 1][k]), bfhi(gu[2 * v][k]) * bfhi(gu[2 * v + 1][k]));
#pragma unroll
          for (int t = 0; t < 4; ++t) { a1[t] = mfma(wf[2 * v][t], y, a1[t]); a2[t] = mfma(wf[2 * v + 1][t], y, a2[t]); }
        }
      }
      *(LAS f4*)(L + L_RED + (w * 2 + 0) * 1024 + lane * 16) = comb2(lo, a1[0], a2[0], a1[1], a2[1]);
      *(LAS f4*)(L + L_RED + (w * 2 + 1) * 1024 + lane * 16) = comb2(lo, a1[2], a2[2], a1[3], a2[3]);
      WG_BARRIER();
      if (w < 2) {
        f4 r[4];
#pragma unroll
        for (int j = 0; j < 4; ++j) r[j] = *(LAS f4*)(L + L_RED + (j * 2 + w) * 1024 + lane * 16);
        __builtin_amdgcn_raw_buffer_store_b128(put_tag((r[0] + r[1]) + (r[2] + r[3]), T), ws,
            WS_DX + (((2 * cj + w) * 4 + g) * 8 + ks) * 256 + (m & 7) * 32 + (m >> 3) * 16, 0, AUX_SC1);
        if (w == 0) STAMP(16);
      }
    }
  }
  STAMP_OUT();
}

extern "C" void kernel_launch(void* const* d_in, const int* in_sizes, int n_in, void* d_out, int out_size, void* d_ws, size_t ws_size,
                              hipStream_t stream) {
  Args a;
  a.x = (const bf16_t*)d_in[0]; a.kc = (const bf16_t*)d_in[1]; a.vc = (const bf16_t*)d_in[2]; a.wq = (const bf16_t*)d_in[3]; a.wo = (const bf16_t*)d_in[4];
  a.wg = (const bf16_t*)d_in[6]; a.wu = (const bf16_t*)d_in[7]; a.wd = (const bf16_t*)d_in[8]; a.out = (bf16_t*)d_out; a.ws = (unsigned char*)d_ws;
  (void)hipFuncSetAttribute((const void*)fused_decode, hipFuncAttributeMaxDynamicSharedMemorySize, LDS_BYTES);
  fused_decode<<<256, 448, LDS_BYTES, stream>>>(a);
}
```

```cpp
#include <hip/hip_runtime.h>
#include <stdint.h>

#define NFL 11
#define NTP 24
#define ADAPT 1
#define LAGQ 4
#define ROT 0
#define NOACT 0
#define TBIAS 0.985f
#define PK 5
#define NTQ 16
#define PDLY 0
#define HDLY 0
#define GDLY 0
#define TLBW 0
#define RMAP 1
#define PXNT 0
#define DXNT 0
#define CKSTEP 12
#define PRNT 0
#define PRE 28
#define POFF 0
#define SOFF 160
#define IDLEDN 1
#define KI 0
#define TPR 1
#define PRONE 0
#define PUBM 0
#define VTOUCH 1
#define WGATE 0
#define PGATE 0
#define XMASK (((h & 4) ? 512 : 0) | ((h & 1) ? 256 : 0) | ((((h >> 1) ^ (h >> 3)) & 1) ? 8 : 0))
#define NINI 3
#define VINI 5
#define CPF 16
#define CPFNT 0
#define CODE_KB 40
#define CPF2 0
#define PLD 0
#define NP0 NFL
#define QP0 18
#define QOFF 0
#define WSLP 1
#define QALL 0
#define DTGT 2
#define SLP_PX0 8
#define SLP_DX0 0
#define QDELAY 4
#define SLP_O 0
#define SLP_P 0
#define SLP_H 0
#define SLP_G 0
#define STAMPS 0

typedef unsigned short bf16_t;
typedef unsigned v4u __attribute__((ext_vector_type(4)));
typedef unsigned v2u __attribute__((ext_vector_type(2)));
typedef float f4 __attribute__((ext_vector_type(4)));
typedef float f2 __attribute__((ext_vector_type(2)));
typedef short v4s __attribute__((ext_vector_type(4)));
typedef short v8s __attribute__((ext_vector_type(8)));
typedef __bf16 bf16x2 __attribute__((ext_vector_type(2)));
typedef __bf16 bf16x8 __attribute__((ext_vector_type(8)));
typedef __amdgpu_buffer_rsrc_t rsrc_t;
#define LAS __attribute__((address_space(3)))
#define FENCE() asm volatile("" ::: "memory")

constexpr int D = 1024, NH = 32, SK = 4096, DH = 128, F = 3584;
constexpr int WS_EP = 0, WS_FL = 2048, WS_PR = 4096, WS_QX = 65536, WS_PX = WS_QX + 256 * 2048, WS_DX = WS_PX + 524288, WS_CLR = WS_DX + 262144;
constexpr int WS_OX = WS_CLR, WS_HX = WS_OX + 65536, WS_GX = WS_HX + 16384;
constexpr int WS_END = WS_GX + 2 * 57344, CLR_PER_WG = (WS_END - WS_CLR) / 256;
static_assert(CLR_PER_WG * 256 == WS_END - WS_CLR && CLR_PER_WG % 128 == 0 && CLR_PER_WG <= 1024, "clear shares");
constexpr int WS_ST = 4 << 20;
constexpr int RT = 9, RINGB = RT * 8192, MISC = 2 * RINGB;
constexpr int C_KL = MISC, C_NK = MISC + 4, C_VL = MISC + 8, C_CS = MISC + 12, C_PK = MISC + 16, C_PV = MISC + 32, C_WQ = MISC + 48, C_XN = MISC + 52, C_QR = MISC + 56,
              C_MG = MISC + 60, C_OS = MISC + 64, C_CD = MISC + 68, C_BD = MISC + 72, C_VF = MISC + 76, C_EP = MISC + 80, C_TS = MISC + 96, C_CT = MISC + 112, C_MS = MISC + 128, C_ER = MISC + 132;
constexpr int L_Q = MISC + 256, L_XN = MISC + 512, XNS = 528, L_MML = MISC + 512, L_MRG = MISC + 1024, L_OF = MISC + 9216, L_RED = MISC + 1024,
              L_CS = MISC + 9216, L_STAT = MISC + 13312, L_DUMP = MISC + 14336;
constexpr int LDS_BYTES = MISC + 16384;
constexpr int AUX_NT = 2, AUX_SC1 = 16;
constexpr int OOB = 0x7fffff00;
constexpr unsigned SENT = 0xffffffffu, TAGM = 0xfu;
constexpr int SPIN_CAP = 1 << 20;
constexpr int PRE_T = 2, VW = 128 + PRE_T, UW = 8 * VW;
constexpr int CKS = CKSTEP, NCK = 120 / CKS, CKD = 5, CKF = (48 + CKS - 1) / CKS;

__device__ __forceinline__ int role_p(int blk) { return 14 * (blk & 7) + (blk >> 3); }
__device__ __forceinline__ int role_q(int blk) { return 16 * (blk & 7) + (blk >> 3) - 14; }
__device__ __forceinline__ float bflo(unsigned u) { return __builtin_bit_cast(float, u << 16); }
__device__ __forceinline__ float bfhi(unsigned u) { return __builtin_bit_cast(float, u & 0xffff0000u); }
__device__ __forceinline__ unsigned pk(float lo, float hi) { bf16x2 v = {(__bf16)lo, (__bf16)hi}; return __builtin_bit_cast(unsigned, v); }
__device__ __forceinline__ float rbf(float f) { return bflo(pk(f, f)); }
__device__ __forceinline__ unsigned nosent(unsigned u) { return u == SENT ? 0x7fc07fc0u : u; }
__device__ __forceinline__ float dot2(unsigned a, unsigned b, float c) {
  return __builtin_amdgcn_fdot2_f32_bf16(__builtin_bit_cast(bf16x2, a), __builtin_bit_cast(bf16x2, b), c, false);
}
template <int CTRL> __device__ __forceinline__ unsigned dppu(unsigned x) { return __builtin_amdgcn_mov_dpp(x, CTRL, 0xf, 0xf, true); }
template <int CTRL> __device__ __forceinline__ float dpp(float x) { return __builtin_bit_cast(float, dppu<CTRL>(__builtin_bit_cast(unsigned, x))); }
constexpr int XOR1 = 0xB1, XOR2 = 0x4E, XOR7 = 0x141, XOR8 = 0x128;
__device__ __forceinline__ float x16_sum(float x) {
  auto s = __builtin_amdgcn_permlane16_swap(__float_as_uint(x), __float_as_uint(x), false, false);
  return __uint_as_float(s[0]) + __uint_as_float(s[1]);
}
__device__ __forceinline__ float x32_sum(float x) {
  auto s = __builtin_amdgcn_permlane32_swap(__float_as_uint(x), __float_as_uint(x), false, false);
  return __uint_as_float(s[0]) + __uint_as_float(s[1]);
}
__device__ __forceinline__ float rows_sum(float x) { return x32_sum(x16_sum(x)); }
__device__ __forceinline__ float row16_sum(float x) { x += dpp<XOR1>(x); x += dpp<XOR2>(x); x += dpp<XOR7>(x); x += dpp<XOR8>(x); return x; }
__device__ __forceinline__ float row16_max(float x) { x = fmaxf(x, dpp<XOR1>(x)); x = fmaxf(x, dpp<XOR2>(x)); x = fmaxf(x, dpp<XOR7>(x)); return fmaxf(x, dpp<XOR8>(x)); }
__device__ __forceinline__ float wave_sum(float x) { return rows_sum(row16_sum(x)); }
__device__ __forceinline__ rsrc_t mkrs(const void* p, unsigned bytes) { return __builtin_amdgcn_make_buffer_rsrc((void*)p, 0, bytes, 0x00020000); }
__device__ __forceinline__ f4 mfma(v4u a, v4u b, f4 c) {
  return __builtin_amdgcn_mfma_f32_16x16x32_bf16(__builtin_bit_cast(bf16x8, a), __builtin_bit_cast(bf16x8, b), c, 0, 0, 0);
}
__device__ __forceinline__ unsigned now32() { return (unsigned)__builtin_amdgcn_s_memrealtime(); }
template <int N> __device__ __forceinline__ void waitvm() { asm volatile("s_waitcnt vmcnt(%0)" ::"n"(N) : "memory"); }
#define WG_BARRIER() asm volatile("s_waitcnt lgkmcnt(0)\n\ts_barrier" ::: "memory")
__device__ __forceinline__ void lds_put(int addr, unsigned v) {
  asm volatile("s_mov_b64 exec, 1\n\tds_write_b32 %0, %1\n\ts_mov_b64 exec, -1" ::"v"(addr), "v"(v) : "memory");
}
__device__ __forceinline__ void lds_max(int addr, unsigned v) {
  asm volatile("s_mov_b64 exec, 1\n\tds_max_u32 %0, %1\n\ts_mov_b64 exec, -1" ::"v"(addr), "v"(v) : "memory");
}
__device__ __forceinline__ unsigned lds_get(int addr) {
  unsigned v; asm volatile("ds_read_b32 %0, %1\n\ts_waitcnt lgkmcnt(0)" : "=v"(v) : "v"(addr) : "memory");
  return __builtin_amdgcn_readfirstlane(v);
}
__device__ __forceinline__ unsigned lds_inc(int addr) {
  unsigned o;
  asm volatile("s_mov_b64 exec, 1\n\tds_add_rtn_u32 %0, %1, %2\n\ts_mov_b64 exec, -1\n\ts_waitcnt lgkmcnt(0)"
               : "=&v"(o) : "v"(addr), "v"(1u) : "memory");
  return __builtin_amdgcn_readfirstlane(o);
}
__device__ __forceinline__ void lds_add(int addr) {
  asm volatile("s_mov_b64 exec, 1\n\tds_add_u32 %0, %1\n\ts_mov_b64 exec, -1" ::"v"(addr), "v"(1u) : "memory");
}
#define lds_wait_ge(addr, thr) do { while (lds_get(addr) < (unsigned)(thr) && --bud > 0) __builtin_amdgcn_s_sleep(1); } while (0)
#define lds_near_ge(addr, thr) do { while (lds_get(addr) < (unsigned)(thr) && --bud > 0) __builtin_amdgcn_s_sleep(WSLP); } while (0)
#define lds_doze_ge(addr, thr) do { while (lds_get(addr) < (unsigned)(thr) && --bud > 0) __builtin_amdgcn_s_sleep(8); } while (0)
#define lds_spin_ge(addr, thr) do { while (lds_get(addr) < (unsigned)(thr) && --bud > 0) {} } while (0)
__device__ __forceinline__ v4u trfrag(LAS unsigned char* L, int a0, int a1) {
  v4s x = __builtin_amdgcn_ds_read_tr16_b64_v4i16((LAS v4s*)(L + a0));
  v4s y = __builtin_amdgcn_ds_read_tr16_b64_v4i16((LAS v4s*)(L + a1));
  return __builtin_bit_cast(v4u, (v8s)__builtin_shufflevector(x, y, 0, 1, 2, 3, 4, 5, 6, 7));
}
__device__ __forceinline__ bool has_sent(v4u v) { return max(max(v.x, v.y), max(v.z, v.w)) == SENT; }
__device__ __forceinline__ v4u mk_tag(v2u E) {
  const unsigned t0 = E.x & TAGM;
  return v4u{t0, ((E.x >> 4) + t0) & TAGM, ((E.x >> 8) + t0) & TAGM, ((E.x >> 12) + t0) & TAGM};
}
__device__ __forceinline__ v4u put_tag(f4 y, v4u T) { return (__builtin_bit_cast(v4u, y) & ~TAGM) | T; }
__device__ __forceinline__ bool bad_tag(v4u v, v4u T) { const v4u d = v ^ T; return ((d.x | d.y | d.z | d.w) & TAGM) != 0u; }
__device__ __forceinline__ f4 strip_tag(v4u v) { return __builtin_bit_cast(f4, v); }
#define GLD(k, POL) "s_mov_b64 exec, %[m" #k "]\n\tbuffer_load_dwordx4 %[v" #k "], %[o" #k "], %[rs], 0 offen " POL "\n\t"
#define GEND "s_mov_b64 exec, -1\n\ts_waitcnt vmcnt(0)"
template <int N, int SLEEP, bool TAGGED, bool NT> __device__ __forceinline__ void gather(v4u rs, const int (&o)[N], v4u (&v)[N], v4u T, int& bud) {
  static_assert(N == 2 || N == 4, "");
  unsigned long long m[N];
#pragma unroll
  for (int j = 0; j < N; ++j) { m[j] = ~0ull; v[j] = v4u{0u, 0u, 0u, 0u}; }
  for (;;) {
    if constexpr (N == 4 && NT)
      asm volatile(GLD(0, "sc1 nt") GLD(1, "sc1 nt") GLD(2, "sc1 nt") GLD(3, "sc1 nt") GEND : [v0] "+v"(v[0]), [v1] "+v"(v[1]), [v2] "+v"(v[2]), [v3] "+v"(v[3])
                   : [o0] "v"(o[0]), [o1] "v"(o[1]), [o2] "v"(o[2]), [o3] "v"(o[3]), [m0] "s"(m[0]), [m1] "s"(m[1]), [m2] "s"(m[2]), [m3] "s"(m[3]), [rs] "s"(rs) : "memory");
    else if constexpr (N == 4)
      asm volatile(GLD(0, "sc1") GLD(1, "sc1") GLD(2, "sc1") GLD(3, "sc1") GEND : [v0] "+v"(v[0]), [v1] "+v"(v[1]), [v2] "+v"(v[2]), [v3] "+v"(v[3])
                   : [o0] "v"(o[0]), [o1] "v"(o[1]), [o2] "v"(o[2]), [o3] "v"(o[3]), [m0] "s"(m[0]), [m1] "s"(m[1]), [m2] "s"(m[2]), [m3] "s"(m[3]), [rs] "s"(rs) : "memory");
    else if constexpr (NT)
      asm volatile(GLD(0, "sc1 nt") GLD(1, "sc1 nt") GEND : [v0] "+v"(v[0]), [v1] "+v"(v[1])
                   : [o0] "v"(o[0]), [o1] "v"(o[1]), [m0] "s"(m[0]), [m1] "s"(m[1]), [rs] "s"(rs) : "memory");
    else
      asm volatile(GLD(0, "sc1") GLD(1, "sc1") GEND : [v0] "+v"(v[0]), [v1] "+v"(v[1])
                   : [o0] "v"(o[0]), [o1] "v"(o[1]), [m0] "s"(m[0]), [m1] "s"(m[1]), [rs] "s"(rs) : "memory");
    unsigned long long any = 0;
    v4u d[N];
#pragma unroll
    for (int j = 0; j < N; ++j) {
      d[j] = TAGGED ? v[j] ^ T : v[j];
      const unsigned long long bb = __builtin_amdgcn_ballot_w64(TAGGED ? ((d[j].x | d[j].y | d[j].z | d[j].w) & TAGM) != 0u : has_sent(v[j]));
      if (TAGGED) asm("s_cmp_lg_u64 %1, 0\n\ts_cselect_b64 %0, -1, 0" : "=s"(m[j]) : "s"(bb) : "scc"); else m[j] = bb;
      any |= bb;
    }
    if (any == 0ull || --bud <= 0) {
#pragma unroll
      for (int j = 0; j < N; ++j) v[j] = d[j];
      break;
    }
    if (SLEEP) __builtin_amdgcn_s_sleep(SLEEP);
  }
}
__device__ __forceinline__ v4u mkrs4(const void* p, unsigned bytes) {
  const unsigned long long u = (unsigned long long)p;
  return v4u{(unsigned)u, (unsigned)(u >> 32) & 0xffffu, bytes, 0x00020000u};
}
#define PIN(x) asm volatile("" : "+v"(x))
template <int N, bool NT = false, bool SC1 = true> __device__ __forceinline__ void touch(v4u rs, const int (&o)[N]) {
#pragma unroll
  for (int j = 0; j < N; ++j) {
    unsigned keep;
    if (!SC1 && NT) asm volatile("s_mov_b32 %0, m0\n\ts_mov_b32 m0, %1\n\ts_nop 0\n\tbuffer_load_dword %2, %3, 0 offen nt lds\n\ts_mov_b32 m0, %0"
                 : "=&s"(keep) : "s"(L_DUMP), "v"(o[j]), "s"(rs) : "memory");
    else if (!SC1) asm volatile("s_mov_b32 %0, m0\n\ts_mov_b32 m0, %1\n\ts_nop 0\n\tbuffer_load_dword %2, %3, 0 offen lds\n\ts_mov_b32 m0, %0"
                 : "=&s"(keep) : "s"(L_DUMP), "v"(o[j]), "s"(rs) : "memory");
    else if (NT) asm volatile("s_mov_b32 %0, m0\n\ts_mov_b32 m0, %1\n\ts_nop 0\n\tbuffer_load_dword %2, %3, 0 offen sc1 nt lds\n\ts_mov_b32 m0, %0"
                 : "=&s"(keep) : "s"(L_DUMP), "v"(o[j]), "s"(rs) : "memory");
    else asm volatile("s_mov_b32 %0, m0\n\ts_mov_b32 m0, %1\n\ts_nop 0\n\tbuffer_load_dword %2, %3, 0 offen sc1 lds\n\ts_mov_b32 m0, %0"
                 : "=&s"(keep) : "s"(L_DUMP), "v"(o[j]), "s"(rs) : "memory");
  }
}
__device__ __forceinline__ f4 comb2(bool lo, f4 a1A, f4 a2A, f4 a1B, f4 a2B) {
  f4 x, y, o;
#pragma unroll
  for (int i = 0; i < 4; ++i) y[i] = lo ? a1B[i] : a2A[i];
#pragma unroll
  for (int i = 0; i < 4; ++i) x[i] = lo ? a1A[i] : a2B[i];
  asm volatile("" : "+v"(x), "+v"(y));
#pragma unroll
  for (int i = 0; i < 4; ++i) o[i] = x[i] + dpp<XOR8>(y[i]);
  return o;
}

struct Args {
  const bf16_t* x; const bf16_t* kc; const bf16_t* vc; const bf16_t* wq; const bf16_t* wo;
  const bf16_t* wg; const bf16_t* wu; const bf16_t* wd; bf16_t* out; unsigned char* ws;
};
__device__ __forceinline__ int region(const Args& a, int blk) {
  constexpr int pk = PK;
  const int r = blk >> 3;
  return (r & ((1 << pk) - 1)) | ((blk & 7) << pk) | ((r >> pk) << (pk + 3));
}

#define MEAS_BASE 12000u
#define MEAS_MUL 4u
#define STAMP(i) do {} while (0)
#define STAMP_OUT() do {} while (0)

__device__ __forceinline__ void dma(rsrc_t rs, LAS unsigned char* L, int lds, int voff, int soff) {
  __builtin_amdgcn_raw_ptr_buffer_load_lds(rs, (LAS void*)(L + lds), 16, voff, soff, 0, AUX_NT);
}

__device__ __forceinline__ void dma_h(v4u rs, int lds, int voff, int soff) {
  unsigned keep;
  asm volatile("s_mov_b32 %0, m0\n\ts_mov_b32 m0, %1\n\ts_nop 0\n\tbuffer_load_dwordx4 %2, %3, %4 offen nt lds\n\ts_mov_b32 m0, %0"
               : "=&s"(keep) : "s"(lds), "v"(voff), "s"(rs), "s"(soff) : "memory");
}

template <int N> __device__ __forceinline__ void wait_pub(int cl, int u, int j) {
  waitvm<N - 1>();
  if (((j - N + 2) & 3) == 0 && u - N + 2 > 0) lds_max(cl, u - N + 2);
}
template <int N> __device__ __forceinline__ void drain_pub(int cl, int n) {
  if constexpr (N > 13) { waitvm<12>(); lds_max(cl, n - 12); }
  if constexpr (N > 9) { waitvm<8>(); lds_max(cl, n - 8); }
  if constexpr (N > 5) { waitvm<4>(); lds_max(cl, n - 4); }
  waitvm<0>(); lds_max(cl, n);
}
template <int X> __device__ __forceinline__ int free_addr(int vt) { const int u = vt - RT - PRE_T; return u < 0 ? C_WQ : C_PK + 16 * (u & 1) + 4 * ((u & 2) + X); }
template <int X> __device__ __forceinline__ unsigned free_thr(int vt) { const int u = vt - RT - PRE_T; return u < 0 ? 2u : (unsigned)((u & ~1) + X + 1); }
template <int X> __device__ __forceinline__ void ring_free(int vt, unsigned have, int& bud) {
  if (have < free_thr<X>(vt)) {
    if (lds_get(free_addr<X>(vt)) < free_thr<X>(vt)) {
      waitvm<0>(); lds_max(X ? C_VL : C_KL, vt * 8);
      lds_wait_ge(free_addr<X>(vt), free_thr<X>(vt));
    }
  }
}
template <int N> __device__ __forceinline__ void kv_tile(rsrc_t rkv, LAS unsigned char* L, int lb, int lane, int so, int cl, int vt) {
  wait_pub<N>(cl, vt * 8, 0);
#pragma unroll
  for (int j = 1; j < 8; ++j) { dma(rkv, L, lb + j * 1024, lane * 16, so ^ (j * 1024)); wait_pub<N>(cl, vt * 8 + j, j); }
}

template <int X> __device__ __forceinline__ void loader(const Args& a, LAS unsigned char* L, int lane, int blk, unsigned t_entry) {
  const int rg = region(a, blk), b = rg >> 5, h = rg & 31, row = blk >> 3;
  constexpr int cl = X ? C_VL : C_KL, ring = X * RINGB;
  int bud = SPIN_CAP;
  unsigned tv = 0; const unsigned t0w = t_entry; (void)t0w;
  const int v128 = (lane >> 3), sw128 = ((lane & 7) ^ (((lane >> 4) & 3) << 1)) * 16;
  const bf16_t* const kp = a.kc + (size_t)(b * NH + h) * SK * DH; const bf16_t* const vp = a.vc + (size_t)(b * NH + h) * SK * DH;
  if (VTOUCH) { const int o1[1] = {lane == 0 ? 0 : OOB}; touch<1, true>(mkrs4(X ? vp : kp, 64), o1); }
  {
    const rsrc_t rs = mkrs(a.wq, D * NH * DH * 2);
    const int voff = v128 * 8192 + sw128, s0 = (256 * (b >> 1) + 128 * X) * 8192 + (h * 128 + 64 * ((b ^ (h >> 3)) & 1)) * 2;
#pragma unroll
    for (int u = 0; u < 8 * PRE_T; ++u) {
      dma(rs, L, ring + u * 1024, voff, s0 + u * 65536);
      if (u == 7) { asm volatile("s_barrier" ::: "memory"); }
      if (u >= NFL + NINI - 1) wait_pub<NFL + NINI>(cl, u, u & 7);
    }
  }
  int st = PRE_T;
  int nsel = NINI, integ = 0;
  int rot = (ROT), xm = (XMASK);
  unsigned have = 0, ck = CKS; v2u ct = {0u, 0u}; unsigned long long t64 = 0;
  for (int vt = PRE_T; vt < VW; ++vt) {
    const rsrc_t rkv = mkrs((vt & 1) ? vp : kp, SK * DH * 2);
    const int lb = ring + st * 8192, so = (((((vt - PRE_T) & ~1) + X + rot) & 127) * 8192) ^ (xm << 10);
    dma(rkv, L, lb, lane * 16, so);
    asm volatile("s_waitcnt lgkmcnt(0)" : "+v"(have), "+v"(ct), "+s"(t64)::"memory");
    const unsigned tn = (unsigned)t64;
    const bool full = vt + 1 >= RT && (unsigned)__builtin_amdgcn_readfirstlane(have) < free_thr<X>(vt + 1);
    if (ADAPT) {
      const unsigned c0 = __builtin_amdgcn_readfirstlane(ct.x), c1 = __builtin_amdgcn_readfirstlane(ct.y);
      if (X == 0) {
        if ((unsigned)vt - 1u == ck) { asm volatile("s_mov_b64 exec, 1\n\tds_write_b64 %0, %1\n\ts_mov_b64 exec, -1" ::"v"(C_TS), "v"(v2u{ck, tn}) : "memory"); ck += CKS; }
        const int lagt = (int)((tn << 8) - c0 - (unsigned)vt * c1) >> 8;
        if (KI && c1 != 0u) integ = max(-(3 << (LAGQ + KI)), min(3 << (LAGQ + KI), integ + max(-64, min(64, lagt - (1 << (LAGQ - 1))))));
        nsel = c1 == 0u ? (vt < VINI ? NINI : 0) : max(-3, min(3, (lagt + (integ >> KI)) >> LAGQ));
        lds_put(C_NK, nsel);
      } else {
        const int d = (int)c0 - 8 * (vt - 1) + NFL - DTGT;
        nsel = vt < 12 ? (vt < VINI ? NINI : 0) : max(-3, min(3, (int)c1 + ((d + 2) >> 2)));
      }
      if (NOACT) nsel = 0;
    } else nsel = vt < VINI ? NINI : 0;
    asm volatile("ds_read_b32 %0, %3\n\tds_read_b64 %1, %4\n\ts_memrealtime %2" : "=&v"(have), "=&v"(ct), "=&s"(t64) : "v"(free_addr<X>(vt + 2)), "v"(X ? C_KL : C_CT) : "memory");
    switch (nsel) {
      case -3: kv_tile<NFL - 3>(rkv, L, lb, lane, so, cl, vt); break;
      case -2: kv_tile<NFL - 2>(rkv, L, lb, lane, so, cl, vt); break;
      case -1: kv_tile<NFL - 1>(rkv, L, lb, lane, so, cl, vt); break;
      case 0: kv_tile<NFL>(rkv, L, lb, lane, so, cl, vt); break;
      case 1: kv_tile<NFL + 1>(rkv, L, lb, lane, so, cl, vt); break;
      case 2: kv_tile<NFL + 2>(rkv, L, lb, lane, so, cl, vt); break;
      default: kv_tile<NFL + 3>(rkv, L, lb, lane, so, cl, vt); break;
    }
    if (full) ring_free<X>(vt + 1, 0u, bud);
    st = st == RT - 1 ? 0 : st + 1;
  }
  asm volatile("s_waitcnt lgkmcnt(0)" : "+v"(have), "+v"(ct), "+s"(t64)::"memory");
  STAMP(8 + X);
  if (PLD && row < 14) {
    const int pi = role_p(blk);
    const rsrc_t rs = mkrs((pi & 1) ? a.wu : a.wg, D * F * 2);
    const int voff = v128 * 7168 + sw128, s0 = 512 * X * 7168 + (pi >> 1) * 128;
#pragma unroll
    for (int v = 0; v < 8; ++v) {
      if (v) ring_free<X>(VW + v, 0u, bud);
#pragma unroll
      for (int j = 0; j < 8; ++j) {
        const int q = v * 8 + j;
        dma(rs, L, ring + ((VW + v) % RT) * 8192 + j * 1024, voff, s0 + q * 57344);
        if (q < QP0) wait_pub<NP0>(cl, UW + q, j); else if (q >= QP0 + NTP - NP0) wait_pub<NTP>(cl, UW + q, j);
      }
    }
    if (NTP > 17) { waitvm<16>(); lds_max(cl, UW + 48); }
    waitvm<8>(); lds_max(cl, UW + 56); waitvm<0>(); lds_max(cl, UW + 64);
  } else if (row >= 14 && row < 30) {
    const int qi = role_q(blk);
    {
      const rsrc_t rs = mkrs(a.wo, NH * DH * D * 2);
      const int r4 = lane >> 4, pos = lane & 15, s0 = (256 * (qi >> 3) + 128 * X) * 2048 + (qi & 7) * 256;
      const int voff0 = r4 * 2048 + (pos ^ (r4 << 1)) * 16, voff1 = r4 * 2048 + (pos ^ ((r4 | 4) << 1)) * 16;
      if (WGATE) {
        const unsigned c0 = lds_get(C_CT), c1 = lds_get(C_CT + 4);
        const unsigned go = c0 + (unsigned)VW * c1 + ((unsigned)(WGATE - 100) << 8);
        if (c1 != 0u && (int)((now32() << 8) - go) < -(30 << 8)) {
          drain_pub<NFL + 3>(cl, UW);
          while ((int)((now32() << 8) - go) < 0 && --bud > 0) __builtin_amdgcn_s_sleep(1);
        }
      }
#pragma unroll
      for (int v = 0; v < 4; ++v) {
        if (v) ring_free<X>(VW + v, 0u, bud);
#pragma unroll
        for (int j = 0; j < 8; ++j) {
          const int q = v * 8 + j;
          dma(rs, L, ring + ((VW + v) % RT) * 8192 + j * 1024, (j & 2) ? voff1 : voff0, s0 + q * 8192);
          wait_pub<NTQ>(cl, UW + q, j);
        }
      }
      if (NTQ > 17) { waitvm<16>(); lds_max(cl, UW + 16); }
      waitvm<8>(); lds_max(cl, UW + 24); waitvm<0>(); lds_max(cl, UW + 32);
    }
  } else {
    drain_pub<NFL + 3>(cl, UW);
  }
  STAMP(10 + X);
  STAMP_OUT();
  asm volatile("s_waitcnt vmcnt(0) lgkmcnt(0)" ::: "memory");
}

__device__ __forceinline__ void ln_params(float s, float ss, float& m, float& inv) {
  const float mean = s * (1.f / D), var = fmaxf(fmaf(-mean, mean, ss * (1.f / D)), 0.f);
  m = rbf(mean);
  inv = __builtin_amdgcn_rcpf(rbf(__builtin_amdgcn_sqrtf(rbf(rbf(var) + 1e-5f))));
}
__device__ __forceinline__ unsigned ln_pair(unsigned u, float m, float inv) { const unsigned d = pk(bflo(u) - m, bfhi(u) - m); return pk(bflo(d) * inv, bfhi(d) * inv); }
__device__ __forceinline__ float sum8(v4u u, float s) { return dot2(u.w, 0x3f803f80u, dot2(u.z, 0x3f803f80u, dot2(u.y, 0x3f803f80u, dot2(u.x, 0x3f803f80u, s)))); }
__device__ __forceinline__ float sq8(v4u u, float s) { return dot2(u.w, u.w, dot2(u.z, u.z, dot2(u.y, u.y, dot2(u.x, u.x, s)))); }

__device__ __forceinline__ void service(const Args& a, LAS unsigned char* L, int lane, int blk, unsigned t_entry, unsigned long long pc) {
  const int row = blk >> 3, g = lane >> 4, m = lane & 15;
  const rsrc_t ws = mkrs(a.ws, WS_END);
  int bud = SPIN_CAP;
  unsigned tv = 0; const unsigned t0w = t_entry; (void)t0w;
  if (blk < CPF) {
    int oc[CODE_KB / 8];
#pragma unroll
    for (int j = 0; j < CODE_KB / 8; ++j) oc[j] = j * 8192 + lane * 128;
    touch<CODE_KB / 8, CPFNT != 0, false>(mkrs4((const void*)(pc & ~255ull), CODE_KB * 1024), oc);
  }
  const v2u ep = __builtin_amdgcn_raw_buffer_load_b64(ws, WS_EP, 0, AUX_SC1);
  ((LAS unsigned*)(L + MISC))[lane] = 0u;
  WG_BARRIER();
  const unsigned long long e = (((unsigned long long)ep.y << 32) | ep.x) + 1ull;
  const v2u E = {(unsigned)e, (unsigned)(e >> 32)};
  if (lane == 0) *(LAS v2u*)(L + C_EP) = E;
  FENCE();
  lds_put(C_ER, 1);
  STAMP(22);
  if (TPR) {
    const int o1[1] = {(blk & 31) == 0 && lane < NCK ? WS_PR + lane * 1024 + (row >> 2) * 128 : ((blk & 15) == 1 && lane == 0 ? WS_FL + blk * 8 : OOB)};
    touch<1>(mkrs4(a.ws, WS_END), o1);
  }
  lds_wait_ge(C_WQ, 2);
  __builtin_amdgcn_raw_buffer_store_b128(v4u{SENT, SENT, SENT, SENT}, ws, lane * 16 < CLR_PER_WG ? WS_CLR + blk * CLR_PER_WG + lane * 16 : OOB, 0, AUX_SC1 | AUX_NT);
  lds_doze_ge(C_KL, 8 * 11);
  waitvm<0>();
  __builtin_amdgcn_raw_buffer_store_b64(E, ws, lane == 0 ? WS_FL + blk * 8 : OOB, 0, AUX_SC1);
  unsigned t_first = 0;
  const int early = row < 14 ? POFF + ((((unsigned)((unsigned long long)((role_p(blk) & 1) ? a.wu : a.wg) >> 7) + (role_p(blk) >> 1)) & 7) == 3 ? SOFF : 0)
                            : (row < 30 && ((((unsigned)((unsigned long long)a.wo >> 7) + 2 * (role_q(blk) & 7)) & 6) == 2 || (((unsigned)((unsigned long long)a.wo >> 7) + 2 * (role_q(blk) & 7) + 1) & 7) == 3) ? QOFF : 0) + (row < 30 ? QALL : 0);
  for (int c = 1; c <= NCK; ++c) {
    if (ADAPT) {
      lds_doze_ge(C_TS, c * CKS);
      const unsigned tc = lds_get(C_TS + 4) + (c > 2 ? early : 0);
      const int pb = WS_PR + (c - 1) * 1024;
      const unsigned ptag = E.x & 255u;
      __builtin_amdgcn_raw_buffer_store_b32((ptag << 24) | (tc & 0xffffffu), ws, lane == 0 && !(row & 3) ? pb + (row >> 2) * 128 + (blk & 7) * 4 : OOB, 0, AUX_SC1);
      lds_doze_ge(C_KL, 8 * (c * CKS + CKD));
      const unsigned f = __builtin_amdgcn_raw_buffer_load_b32(ws, pb + (lane >> 3) * 128 + (lane & 7) * 4, 0, AUX_SC1);
      const int late = (int)(now32() - tc), d = (int)((f - tc) << 8) >> 8;
      const float sb = wave_sum((float)(((f >> 24) == ptag && d > -4000 && d < late) ? d : late));
      const unsigned tm = tc + (unsigned)(int)(sb * (1.f / 64));
      if (c == 1) t_first = tm;
      else if (lane == 0) {
        const unsigned tau = (unsigned)((float)(tm - t_first) * (256.f * TBIAS / CKS) / (float)(c - 1));
        *(LAS v2u*)(L + C_CT) = v2u{((tm - early) << 8) - (unsigned)(c * CKS + 1) * tau - (128u << LAGQ), tau};
      }
    } else lds_doze_ge(C_KL, 8 * (c * CKS + CKD));
    if (TLBW && c == 2 && row < 30) {
      const int pi = role_p(blk), qi = role_q(blk);
      unsigned t;
      if (row < 14) t = __builtin_amdgcn_raw_buffer_load_b32(mkrs((pi & 1) ? a.wu : a.wg, D * F * 2), lane < 8 ? lane * 128 * 7168 + (pi >> 1) * 128 : OOB, 0, AUX_NT);
      else {
        t = __builtin_amdgcn_raw_buffer_load_b32(mkrs(a.wo, NH * DH * D * 2), lane < 2 ? (256 * (qi >> 3) + 255 * lane) * 2048 + (qi & 7) * 256 : OOB, 0, AUX_NT);
        t += __builtin_amdgcn_raw_buffer_load_b32(mkrs(a.wd, F * D * 2), lane < 2 ? (448 * (qi >> 4) + 447 * lane) * 2048 + (qi & 15) * 128 : OOB, 0, AUX_NT);
        __builtin_amdgcn_raw_buffer_store_b64(v2u{0u, 0u}, mkrs(a.out, 8 * D * 2), g == 0 ? ((m >> 1) * D + 32 * (qi >> 2) + 16 * (m & 1) + 4 * (qi & 3)) * 2 : OOB, 0, 0);
      }
      asm volatile("" ::"v"(t));
    }
    if (CPF2 && c == NCK && blk < CPF2) {
      int oc[CODE_KB / 8];
#pragma unroll
      for (int j = 0; j < CODE_KB / 8; ++j) oc[j] = j * 8192 + lane * 128;
      touch<CODE_KB / 8, true, false>(mkrs4((const void*)(pc & ~255ull), CODE_KB * 1024), oc);
    }
    if (c == CKF) {
      for (;;) {
        FENCE();
        const v4u f0 = __builtin_amdgcn_raw_buffer_load_b128(ws, WS_FL + lane * 16, 0, AUX_SC1);
        const v4u f1 = __builtin_amdgcn_raw_buffer_load_b128(ws, WS_FL + 1024 + lane * 16, 0, AUX_SC1);
        const bool ok = (f0.x == E.x) & (f0.y == E.y) & (f0.z == E.x) & (f0.w == E.y) & (f1.x == E.x) & (f1.y == E.y) & (f1.z == E.x) & (f1.w == E.y);
        if (__all(ok) || --bud <= 0) break;
        __builtin_amdgcn_s_sleep(32);
      }
      if (blk == 0) __builtin_amdgcn_raw_buffer_store_b64(E, ws, lane == 0 ? WS_EP : OOB, 0, AUX_SC1);
      lds_put(C_VF, 1);
    }
  }
  if (row < 14 || row >= 30) { STAMP_OUT(); return; }
  const int qi = role_q(blk), Tg = qi >> 2, gR = qi & 3;
  const v4u T = mk_tag(E);
  const int xo = ((m >> 1) * D + 32 * Tg + 16 * (m & 1) + 4 * gR) * 2;
  const v2u xres = __builtin_amdgcn_raw_buffer_load_b64(mkrs(a.x, 8 * D * 2), xo, 0, 0);
  float hres[4];
  const v4u ws4 = mkrs4(a.ws, WS_END);
  {
    int off[4], lo_[4]; v4u ov[4];
#pragma unroll
    for (int i = 0; i < 4; ++i) {
      off[i] = WS_OX + ((qi >> 3) * 16 + 4 * i + g) * 256 + m * 16;
      const int Tk = 4 * (i >> 1) + (m >> 2);
      lo_[i] = L_OF + (Tk >> 1) * 1024 + (16 * (m & 3) + 8 * (Tk & 1) + ((4 * i + g) & 7)) * 16;
      PIN(off[i]); PIN(lo_[i]);
    }
    if (PRE & 1) touch<4>(ws4, off);
    lds_wait_ge(C_MG, 4);
    if (PRE & 2) { int o2[4]; for (int i = 0; i < 4; ++i) o2[i] = WS_PX + qi * 4096 + i * 1024 + lane * 16; touch<4, (PXNT & 2) != 0>(ws4, o2); }
    gather<4, SLP_O, false, false>(ws4, off, ov, T, bud);
#pragma unroll
    for (int i = 0; i < 4; ++i) *(LAS v4u*)(L + lo_[i]) = ov[i];
    FENCE();
    lds_put(C_OS, 1);
    STAMP(6);
  }
  {
    int off[4]; v4u pv[4];
#pragma unroll
    for (int i = 0; i < 4; ++i) { off[i] = WS_PX + qi * 4096 + i * 1024 + lane * 16; PIN(off[i]); }
    lds_spin_ge(C_BD, 4);
    __builtin_amdgcn_s_sleep(SLP_PX0);
    gather<4, SLP_P, true, (PXNT & 2) != 0>(ws4, off, pv, T, bud);
    STAMP(12);
    const f4 s = (strip_tag(pv[0]) + strip_tag(pv[1])) + (strip_tag(pv[2]) + strip_tag(pv[3]));
    const float xs[4] = {bflo(xres.x), bfhi(xres.x), bflo(xres.y), bfhi(xres.y)};
#pragma unroll
    for (int i = 0; i < 4; ++i) hres[i] = xs[i] + rbf(rows_sum(s[i]));
    const v2u hp = {pk(hres[0], hres[1]), pk(hres[2], hres[3])};
    __builtin_amdgcn_raw_buffer_store_b64(v2u{nosent(hp.x), nosent(hp.y)}, ws,
        g == 0 ? WS_HX + (Tg >> 1) * 1024 + (16 * gR + 8 * (Tg & 1)) * 16 + m * 8 : OOB, 0, AUX_SC1);
    lds_put(C_CD, now32() | 1u);
    hres[0] = bflo(hp.x); hres[1] = bfhi(hp.x); hres[2] = bflo(hp.y); hres[3] = bfhi(hp.y);
    if (PRE & 4) { int o2[2]; for (int i = 0; i < 2; ++i) o2[i] = WS_DX + qi * 2048 + i * 1024 + lane * 16; touch<2, (DXNT & 2) != 0>(ws4, o2); }
    STAMP(13);
  }
  WG_BARRIER();
  {
    int off[2]; v4u pv[2];
#pragma unroll
    for (int i = 0; i < 2; ++i) off[i] = WS_DX + qi * 2048 + i * 1024 + lane * 16;
    __builtin_amdgcn_s_sleep(SLP_DX0);
    gather<2, 0, true, (DXNT & 2) != 0>(ws4, off, pv, T, bud);
    STAMP(17);
    const f4 s = strip_tag(pv[0]) + strip_tag(pv[1]);
    float o[4];
#pragma unroll
    for (int i = 0; i < 4; ++i) o[i] = hres[i] + rbf(rows_sum(s[i]));
    __builtin_amdgcn_raw_buffer_store_b64(v2u{pk(o[0], o[1]), pk(o[2], o[3])}, mkrs(a.out, 8 * D * 2), g == 0 ? xo : OOB, 0, 0);
    STAMP(18);
  }
  STAMP_OUT();
}

__global__ void __launch_bounds__(448, 2) fused_decode(Args a) {
  unsigned long long pc;
  asm volatile("s_getpc_b64 %0" : "=s"(pc));
  unsigned t_entry = 0;
  extern __shared__ __attribute__((aligned(16))) unsigned char smem[];
  LAS unsigned char* L = (LAS unsigned char*)smem;
  const int tid = threadIdx.x, lane = tid & 63, wave = __builtin_amdgcn_readfirstlane(tid >> 6), blk = blockIdx.x;
  if (wave == 0) { __builtin_amdgcn_s_setprio(3); loader<0>(a, L, lane, blk, t_entry); return; }
  if (wave == 1) { __builtin_amdgcn_s_setprio(3); loader<1>(a, L, lane, blk, t_entry); return; }
  if (wave == 6) { service(a, L, lane, blk, t_entry, pc); return; }

  const int w = wave - 2, rg = region(a, blk), b = rg >> 5, h = rg & 31, row = blk >> 3;
  const int g = lane >> 4, m = lane & 15, q4 = (lane >> 2) & 3, p4 = lane & 3;
  const bool lo = m < 8;
  int bud = SPIN_CAP;
  unsigned tv = 0; const unsigned t0w = t_entry; (void)t0w;
  const rsrc_t ws = mkrs(a.ws, WS_END);
  const v4u ws4 = mkrs4(a.ws, WS_END);
  const int r8 = 4 * (g & 1) + q4;
  const int sw128 = ((lane & 7) ^ (((lane >> 4) & 3) << 1)) * 16;
  const int ta0 = (g >> 1) * 1024 + r8 * 128 + 8 * p4;
  v4u T;
  {
    const rsrc_t rx = mkrs(a.x, 8 * D * 2);
    v4u xr[2][2];
#pragma unroll
    for (int i = 0; i < 2; ++i)
#pragma unroll
      for (int j = 0; j < 2; ++j) xr[i][j] = __builtin_amdgcn_raw_buffer_load_b128(rx, (2 * w + i) * 2048 + j * 1024 + lane * 16, 0, 0);
    WG_BARRIER();
    if (w == 3) STAMP(30);
    asm volatile("" : "+v"(xr[0][0]), "+v"(xr[1][1]));
    if (w == 3) STAMP(31);
    const int kq = b >> 1;
#pragma unroll
    for (int i = 0; i < 2; ++i) {
      float mu, inv;
      ln_params(wave_sum(sum8(xr[i][1], sum8(xr[i][0], 0.f))), wave_sum(sq8(xr[i][1], sq8(xr[i][0], 0.f))), mu, inv);
      const v4u u = (kq & 2) ? xr[i][1] : xr[i][0];
      const v4u y = {ln_pair(u.x, mu, inv), ln_pair(u.y, mu, inv), ln_pair(u.z, mu, inv), ln_pair(u.w, mu, inv)};
      if ((lane >> 5) == (kq & 1)) *(LAS v4u*)(L + L_XN + (2 * w + i) * XNS + (lane & 31) * 16) = y;
    }
    FENCE();
    lds_add(C_XN);
    if (w < 2) {
      lds_spin_ge(C_XN, 4);
      if (w == 0) STAMP(20);
      v4u xb[4];
#pragma unroll
      for (int s = 0; s < 4; ++s) {
        const int o = L_XN + (m & 7) * XNS + (32 * (2 * s + (m >> 3)) + 4 * g) * 2;
        const v2u u0 = *(LAS v2u*)(L + o), u1 = *(LAS v2u*)(L + o + 32);
        xb[s] = v4u{u0.x, u0.y, u1.x, u1.y};
      }
      f4 a1[2], a2[2];
      a1[0] = a1[1] = a2[0] = a2[1] = f4{0.f, 0.f, 0.f, 0.f};
      lds_spin_ge(C_KL, 16); lds_spin_ge(C_VL, 16);
      if (w == 0) STAMP(1);
#pragma unroll
      for (int s = 0; s < 4; ++s)
#pragma unroll
        for (int t = 0; t < 2; ++t) {
          const int base = (s >> 1) * RINGB + (s & 1) * 8192 + ta0 + (((2 * w + t) ^ (r8 >> 1)) * 32);
          a1[t] = mfma(trfrag(L, base, base + 2048), xb[s], a1[t]);
          a2[t] = mfma(trfrag(L, base + 4096, base + 6144), xb[s], a2[t]);
        }
      lds_spin_ge(C_ER, 1);
      T = mk_tag(*(LAS v2u*)(L + C_EP));
      __builtin_amdgcn_raw_buffer_store_b128(put_tag(comb2(lo, a1[0], a2[0], a1[1], a2[1]), T), ws,
          WS_QX + ((h * 8 + (m & 7)) * 4 + kq) * 512 + (((b ^ (h >> 3)) & 1) * 16 + (2 * w + (m >> 3)) * 4 + g) * 16, 0, AUX_SC1 | AUX_NT);
      FENCE();
      lds_add(C_WQ);
      if (w == 0) STAMP(21);
    } else if (w == 2) {
      lds_wait_ge(C_WQ, 2);
      T = mk_tag(*(LAS v2u*)(L + C_EP));
      for (int i = 0; i < QDELAY; ++i) __builtin_amdgcn_s_sleep(8);
      int off[2]; v4u t[2];
#pragma unroll
      for (int j = 0; j < 2; ++j) off[j] = WS_QX + (h * 8 + b) * 2048 + j * 1024 + lane * 16;
      gather<2, 4, true, true>(ws4, off, t, T, bud);
      const f4 s = strip_tag(t[0]) + strip_tag(t[1]);
      const v2u qp = {pk(x32_sum(s[0]), x32_sum(s[1])), pk(x32_sum(s[2]), x32_sum(s[3]))};
      if (lane < 32) *(LAS v2u*)(L + L_Q + lane * 8) = qp;
      FENCE();
      lds_put(C_QR, 1);
      STAMP(2);
    }
    lds_wait_ge(C_QR, 1);
    T = mk_tag(*(LAS v2u*)(L + C_EP));
  }
  {
    float acc[8], mx = -INFINITY, l = 0.f;
    const v4u qv = *(LAS v4u*)(L + L_Q + m * 16);
#pragma unroll
    for (int k = 0; k < 8; ++k) acc[k] = 0.f;
    const int cl = (w & 1) ? C_VL : C_KL;
    int amm = L_MML + m * 8, amr = L_MRG + lane * 8, aox = WS_OX + ((h >> 1) * 16 + (h & 1) * 8 + b) * 256 + lane * 4, amw = L_MRG + (w * 4 + g) * 512 + m * 32;
    PIN(amm); PIN(amr); PIN(aox); PIN(amw);
    int sk = PRE_T + (w & 2);
    for (int it = 0; it < 32; ++it) {
      const int t = 4 * it + w;
      if (it == 28) lds_wait_ge(C_VF, 1);
      const int u0 = 8 * (PRE_T + (t & ~1));
      lds_wait_ge(cl, u0 + 8);
      const int ok = (w & 1) * RINGB + sk * 8192 + lane * 16, ov = (w & 1) * RINGB + (sk == RT - 1 ? 0 : sk + 1) * 8192 + lane * 16;
      float x[8];
      {
        v4u k[8];
#pragma unroll
        for (int i = 0; i < 8; ++i) k[i] = *(LAS v4u*)(L + ok + i * 1024);
#pragma unroll
        for (int i = 0; i < 8; ++i) x[i] = dot2(k[i].w, qv.w, dot2(k[i].z, qv.z, dot2(k[i].y, qv.y, dot2(k[i].x, qv.x, 0.f))));
      }
#define TR_STEP(HALF, CTRL, BIT) _Pragma("unroll") for (int hh = 0; hh < (HALF); ++hh) { \
        const float lo_ = x[hh], hi_ = x[hh + (HALF)]; const bool up_ = m & (BIT); \
        x[hh] = (up_ ? hi_ : lo_) + dpp<CTRL>(up_ ? lo_ : hi_); }
      TR_STEP(4, XOR8, 8) TR_STEP(2, XOR7, 4) TR_STEP(1, XOR2, 2)
#undef TR_STEP
      float s = x[0] + dpp<XOR1>(x[0]);
      asm volatile("" : "+v"(s));
      lds_put(C_PK + 4 * w, t + 1);
      s = rbf(rbf(s) * 0.08838834764831845f) * 1.4426950408889634f;
      float pm = fmaxf(s, dpp<XOR2>(s)); pm = fmaxf(pm, dpp<XOR7>(pm)); pm = fmaxf(pm, dpp<XOR8>(pm));
      const float mn = fmaxf(mx, pm);
      const float alpha = __builtin_amdgcn_exp2f(mx - mn), p = __builtin_amdgcn_exp2f(s - mn);
      l = fmaf(l, alpha, p); mx = mn;
      unsigned pp = pk(p, p), plo = pp & 0xffffu, phi = pp & 0xffff0000u;
#pragma unroll
      for (int k = 0; k < 8; ++k) acc[k] *= alpha;
#define PIN_ACC() asm volatile("" : "+v"(acc[0]), "+v"(acc[1]), "+v"(acc[2]), "+v"(acc[3]), "+v"(acc[4]), "+v"(acc[5]), "+v"(acc[6]), "+v"(acc[7]), "+v"(plo), "+v"(phi))
#define PV4(i0) { v4u v[4]; _Pragma("unroll") for (int i = 0; i < 4; ++i) v[i] = *(LAS v4u*)(L + ov + ((i0) + i) * 1024); \
        const unsigned a_[4] = {dppu<0x150 + 2 * (i0)>(plo), dppu<0x152 + 2 * (i0)>(plo), dppu<0x154 + 2 * (i0)>(plo), dppu<0x156 + 2 * (i0)>(plo)}; \
        const unsigned b_[4] = {dppu<0x150 + 2 * (i0)>(phi), dppu<0x152 + 2 * (i0)>(phi), dppu<0x154 + 2 * (i0)>(phi), dppu<0x156 + 2 * (i0)>(phi)}; \
        _Pragma("unroll") for (int i = 0; i < 4; ++i) _Pragma("unroll") for (int k = 0; k < 4; ++k) { \
          acc[2 * k] = dot2(v[i][k], a_[i], acc[2 * k]); acc[2 * k + 1] = dot2(v[i][k], b_[i], acc[2 * k + 1]); } }
      PIN_ACC();
      lds_near_ge(cl, u0 + 12);
      PV4(0)
      PIN_ACC();
      lds_near_ge(cl, u0 + 16);
      PV4(4)
      PIN_ACC();
#undef PV4
      lds_put(C_PV + 4 * w, t + 1);
      sk = sk >= RT - 4 ? sk - (RT - 4) : sk + 4;
    }
    if (w == 3) STAMP(3);
    *(LAS f4*)(L + amw) = f4{acc[0], acc[1], acc[2], acc[3]};
    *(LAS f4*)(L + amw + 16) = f4{acc[4], acc[5], acc[6], acc[7]};
    l += dpp<XOR2>(l); l += dpp<XOR7>(l); l += dpp<XOR8>(l);
    *(LAS f2*)(L + L_MML + (w * 4 + g) * 8) = f2{mx, l};
    FENCE();
    if (lds_inc(C_MG) == 3) {
      const f2 ml = *(LAS f2*)(L + amm);
      f2 r[16];
#pragma unroll
      for (int j = 0; j < 16; ++j) r[j] = *(LAS f2*)(L + amr + j * 512);
      __builtin_amdgcn_sched_barrier(0);
      float M = ml.x;
#define DPPOP(op, x, ctl) asm volatile("s_nop 1\n\t" op " %0, %0, %0 " ctl " row_mask:0xf bank_mask:0xf" : "+v"(x))
      DPPOP("v_max_f32_dpp", M, "quad_perm:[1,0,3,2]"); DPPOP("v_max_f32_dpp", M, "quad_perm:[2,3,0,1]");
      DPPOP("v_max_f32_dpp", M, "row_half_mirror"); DPPOP("v_max_f32_dpp", M, "row_ror:8");
      float e = __builtin_amdgcn_exp2f(ml.x - M), ls = ml.y * e;
      float o0 = 0.f, o1 = 0.f, o2 = 0.f, o3 = 0.f;
      asm volatile("s_nop 0" : "+v"(e), "+v"(ls), "+v"(o0), "+v"(o1), "+v"(o2), "+v"(o3));
#define FMB(o, x, j) asm volatile("v_fmac_f32_dpp %0, %1, %2 row_newbcast:" #j " row_mask:0xf bank_mask:0xf" : "+v"(o) : "v"(e), "v"(x))
#define MG(j, k) FMB(o0, r[j].x, j); FMB(o1, r[j].y, j); FMB(o2, r[k].x, k); FMB(o3, r[k].y, k);
#define LS(ctl) asm volatile("v_add_f32_dpp %0, %0, %0 " ctl " row_mask:0xf bank_mask:0xf" : "+v"(ls));
      MG(0, 1) LS("quad_perm:[1,0,3,2]") MG(2, 3) LS("quad_perm:[2,3,0,1]") MG(4, 5) LS("row_half_mirror") MG(6, 7) LS("row_ror:8")
      MG(8, 9)
      const float inv = __builtin_amdgcn_rcpf(ls);
      MG(10, 11) MG(12, 13) MG(14, 15)
#undef FMB
#undef MG
#undef LS
#undef DPPOP
      __builtin_amdgcn_raw_buffer_store_b32(nosent(pk((o0 + o2) * inv, (o1 + o3) * inv)), ws, aox, 0, AUX_SC1);
      STAMP(4);
    }
  }
  const unsigned t_att = HDLY ? now32() : 0u;
  if (row < 14) {
    const int pi = role_p(blk);
    v4u wf[8][4];
    f4 a1[4], a2[4];
#pragma unroll
    for (int t = 0; t < 4; ++t) a1[t] = f4{0.f, 0.f, 0.f, 0.f};
    {
      const v4u rw = mkrs4((pi & 1) ? a.wu : a.wg, D * F * 2);
      const int voff = (lane >> 3) * 7168 + sw128, s0 = 256 * w * 7168 + (pi >> 1) * 128;
      if (PRE & 8) { int o2[4]; for (int i = 0; i < 4; ++i) o2[i] = WS_HX + (4 * w + i) * 1024 + lane * 16; touch<4>(ws4, o2); }
      if (!PLD) lds_wait_ge(C_MG, 4);
      if (PDLY) __builtin_amdgcn_s_sleep(PDLY);
      if (PGATE) {
        const unsigned c0 = lds_get(C_CT), c1 = lds_get(C_CT + 4);
        const unsigned go = c0 + (unsigned)VW * c1 + ((unsigned)PGATE << 8);
        if (c1 != 0u) while ((int)((now32() << 8) - go) < 0 && --bud > 0) __builtin_amdgcn_s_sleep(1);
      }
#pragma unroll
      for (int q = 0; q < 32; ++q) if (!PLD) dma_h(rw, w * 32768 + q * 1024, voff, s0 + q * 57344);
    }
#pragma unroll
    for (int v = 0; v < 4; ++v) {
      if (PLD) lds_wait_ge((w & 2) ? C_VL : C_KL, UW + 8 * (4 * (w & 1) + v + 1));
      else if (v == 0) waitvm<24>(); else if (v == 1) waitvm<16>(); else if (v == 2) waitvm<8>(); else waitvm<0>();
      const int sb = PLD ? (w >> 1) * RINGB + ((w & 1) ? (VW + 4 + v) % RT : (VW + v) % RT) * 8192 : w * 32768 + v * 8192;
#pragma unroll
      for (int hs = 0; hs < 2; ++hs)
#pragma unroll
        for (int t = 0; t < 4; ++t) {
          const int ad = sb + hs * 4096 + ta0 + ((t ^ (r8 >> 1)) * 32);
          wf[2 * v + hs][t] = trfrag(L, ad, ad + 2048);
          a1[t] = mfma(wf[2 * v + hs][t], v4u{0x3f803f80u, 0x3f803f80u, 0x3f803f80u, 0x3f803f80u}, a1[t]);
        }
    }
    *(LAS f4*)(L + L_CS + w * 512 + (g * 2 + (m >> 3)) * 64 + (m & 1) * 16) = (m & 1) ? (lo ? a1[2] : a1[3]) : (lo ? a1[0] : a1[1]);
    FENCE();
    lds_add(C_CS);
    int off[4]; v4u hv[4];
#pragma unroll
    for (int s = 0; s < 4; ++s) { off[s] = WS_HX + (4 * w + s) * 1024 + lane * 16; PIN(off[s]); }
    int og = WS_GX + (pi & 1) * 57344 + (pi >> 1) * 1024 + (16 * g + 8 * w + (m & 7)) * 16 + (m >> 3) * 8;
    int lrw = L_RED + w * 2048 + lane * 16, lrr = L_RED + (w & 1) * 1024 + lane * 16, lsw = L_STAT + ((m & 7) * 4 + w) * 8, lsr = L_STAT + (m & 7) * 32;
    PIN(og); PIN(lrw); PIN(lrr); PIN(lsw); PIN(lsr);
    lds_spin_ge(C_CS, 4);
    f4 cs;
    {
      f4 c[4];
#pragma unroll
      for (int j = 0; j < 4; ++j) c[j] = *(LAS f4*)(L + L_CS + j * 512 + (g * 2 + (m >> 3)) * 64 + (w & 1) * 16);
      cs = (c[0] + c[1]) + (c[2] + c[3]);
      PIN(cs);
    }
    if (w == 0) STAMP(5);
    if (HDLY) { while ((int)(now32() - t_att) < HDLY && --bud > 0) __builtin_amdgcn_s_sleep(1); }
    gather<4, SLP_H, false, false>(ws4, off, hv, T, bud);
    if (w == 0) STAMP(6);
#pragma unroll
    for (int t = 0; t < 4; ++t) a1[t] = a2[t] = f4{0.f, 0.f, 0.f, 0.f};
    float sm = sum8(hv[1], sum8(hv[0], 0.f)) + sum8(hv[3], sum8(hv[2], 0.f));
    float s2 = sq8(hv[1], sq8(hv[0], 0.f)) + sq8(hv[3], sq8(hv[2], 0.f));
    sm += dpp<XOR8>(sm); s2 += dpp<XOR8>(s2);
    sm = rows_sum(sm); s2 = rows_sum(s2);
    *(LAS f2*)(L + lsw) = f2{sm, s2};
#pragma unroll
    for (int tp = 0; tp < 4; tp += 2)
#pragma unroll
      for (int s = 0; s < 4; ++s)
#pragma unroll
        for (int t = tp; t < tp + 2; ++t) { a1[t] = mfma(wf[2 * s][t], hv[s], a1[t]); a2[t] = mfma(wf[2 * s + 1][t], hv[s], a2[t]); }
#pragma unroll
    for (int i = 0; i < 32; ++i) { __builtin_amdgcn_sched_group_barrier(0x8, 1, 0); __builtin_amdgcn_sched_group_barrier(0x2, 3, 0); }
    *(LAS f4*)(L + lrw) = comb2(lo, a1[0], a2[0], a1[1], a2[1]);
    *(LAS f4*)(L + lrw + 1024) = comb2(lo, a1[2], a2[2], a1[3], a2[3]);
    WG_BARRIER();
    if (w < 2) {
      f4 r[4], sp[2];
#pragma unroll
      for (int j = 0; j < 2; ++j) sp[j] = *(LAS f4*)(L + lsr + j * 16);
#pragma unroll
      for (int j = 0; j < 4; ++j) r[j] = *(LAS f4*)(L + lrr + j * 2048);
      __builtin_amdgcn_sched_barrier(0);
      const f4 sq = sp[0] + sp[1];
      float mu, inv; ln_params(sq[0] + sq[2], sq[1] + sq[3], mu, inv);
      f4 y = (((r[0] + r[1]) + (r[2] + r[3])) - mu * cs) * inv;
      if (!(pi & 1)) {
#pragma unroll
        for (int i = 0; i < 4; ++i) {
          const float gg = rbf(y[i]);
          const float z = gg * fmaf(0.044715f * gg, gg, 1.f);
          y[i] = gg * __builtin_amdgcn_rcpf(1.f + __builtin_amdgcn_exp2f(-2.3022081982f * z));
        }
      }
      __builtin_amdgcn_raw_buffer_store_b64(v2u{nosent(pk(y[0], y[1])), nosent(pk(y[2], y[3]))}, ws, og, 0, AUX_SC1);
      if (w == 0) STAMP(7);
    }
  } else {
    const int qi = role_q(blk);
    if (row < 30) {
      v4u wf[8][2];
      const int tb = 2 * g * 1024 + q4 * 256 + 8 * p4, fx = q4 | ((g & 1) << 2);
#pragma unroll
      for (int Tk = 0; Tk < 8; ++Tk) {
        lds_wait_ge(Tk >= 4 ? C_VL : C_KL, 8 * (VW + (Tk & 3) + 1));
#pragma unroll
        for (int t = 0; t < 2; ++t) { const int ad = (Tk >> 2) * RINGB + ((VW + (Tk & 3)) % RT) * 8192 + tb + (((2 * w + t) ^ fx) * 32); wf[Tk][t] = trfrag(L, ad, ad + 1024); }
      }
      if (w == 0) STAMP(5);
#pragma unroll
      for (int i = 0; i < 16; ++i) PIN(wf[i >> 1][i & 1]);
      int op = WS_PX + (((4 * (qi & 7) + w) * 4 + g) * 16 + (qi >> 3)) * 256 + (m & 7) * 32 + (m >> 3) * 16;
      PIN(op);
      lds_spin_ge(C_OS, 1);
      f4 a1[2], a2[2];
      a1[0] = a1[1] = a2[0] = a2[1] = f4{0.f, 0.f, 0.f, 0.f};
#pragma unroll
      for (int s = 0; s < 4; ++s) {
        const v4u y = *(LAS v4u*)(L + L_OF + s * 1024 + lane * 16);
#pragma unroll
        for (int t = 0; t < 2; ++t) { a1[t] = mfma(wf[2 * s][t], y, a1[t]); a2[t] = mfma(wf[2 * s + 1][t], y, a2[t]); }
      }
      __builtin_amdgcn_raw_buffer_store_b128(put_tag(comb2(lo, a1[0], a2[0], a1[1], a2[1]), T), ws, op, 0, AUX_SC1 | ((PXNT & 1) ? AUX_NT : 0));
      lds_add(C_BD);
      if (w == 0) STAMP(7);
    }
    const int cslow = (3 - (int)((unsigned long long)a.wd >> 7)) & 7;
    if (row >= 30 ? IDLEDN : (!IDLEDN || (qi & 7) != cslow)) {
      const int ks = row < 30 ? qi >> 4 : blk & 7, cj = row < 30 ? qi & 15 : cslow + 8 * (row - 30);
      v4u wf[4][4];
      {
        const v4u rw = mkrs4(a.wd, F * D * 2);
        const int voff = (lane >> 3) * 2048 + sw128, s0 = (448 * ks + 128 * w) * 2048 + cj * 128;
        lds_wait_ge(row < 30 ? C_CD : C_MG, row < 30 ? 1 : 4);
        if (PRE & 16) { int o2[4]; for (int i = 0; i < 4; ++i) o2[i] = (i < 2 || w < 3) ? WS_GX + (i & 1) * 57344 + (7 * ks + 2 * w + (i >> 1)) * 1024 + lane * 16 : OOB; touch<4>(ws4, o2); }
#pragma unroll
        for (int q = 0; q < 16; ++q) if (q < 8 || w < 3) dma_h(rw, w * 16384 + q * 1024, voff, s0 + q * 16384);
        waitvm<0>();
      }
#pragma unroll
      for (int v = 0; v < 2; ++v) {
        const int sb = w * 16384 + (w < 3 ? v * 8192 : 0);
#pragma unroll
        for (int hs = 0; hs < 2; ++hs)
#pragma unroll
          for (int t = 0; t < 4; ++t) { const int ad = sb + hs * 4096 + ta0 + ((t ^ (r8 >> 1)) * 32); wf[2 * v + hs][t] = trfrag(L, ad, ad + 2048); }
      }
#pragma unroll
      for (int i = 0; i < 16; ++i) PIN(wf[i >> 2][i & 3]);
      if (w == 0) STAMP(14);
      int off[4]; v4u gu[4];
#pragma unroll
      for (int v = 0; v < 2; ++v) {
        const bool live = v == 0 || w < 3;
        off[2 * v] = live ? WS_GX + (7 * ks + 2 * w + v) * 1024 + lane * 16 : OOB;
        off[2 * v + 1] = live ? off[2 * v] + 57344 : OOB;
      }
      int od = WS_DX + (((2 * cj + w) * 4 + g) * 8 + ks) * 256 + (m & 7) * 32 + (m >> 3) * 16;
      int lrw = L_RED + w * 2048 + lane * 16, lrr = L_RED + (w & 1) * 1024 + lane * 16;
      PIN(od); PIN(lrw); PIN(lrr); PIN(off[0]); PIN(off[1]); PIN(off[2]); PIN(off[3]);
      if (GDLY) { const unsigned tc = lds_get(C_CD); while ((int)(now32() - tc) < GDLY && --bud > 0) __builtin_amdgcn_s_sleep(1); }
      gather<4, SLP_G, false, false>(ws4, off, gu, T, bud);
      if (w == 0) STAMP(15);
      f4 a1[4], a2[4];
#pragma unroll
      for (int t = 0; t < 4; ++t) a1[t] = a2[t] = f4{0.f, 0.f, 0.f, 0.f};
#pragma unroll
      for (int v = 0; v < 2; ++v) {
        {
          v4u y;
#pragma unroll
          for (int k = 0; k < 4; ++k) {
            const f2 pr = f2{bflo(gu[2 * v][k]), bfhi(gu[2 * v][k])} * f2{bflo(gu[2 * v + 1][k]), bfhi(gu[2 * v + 1][k])};
            y[k] = pk(pr.x, pr.y);
          }
#pragma unroll
          for (int t = 0; t < 4; ++t) { a1[t] = mfma(wf[2 * v][t], y, a1[t]); a2[t] = mfma(wf[2 * v + 1][t], y, a2[t]); }
        }
      }
      __builtin_amdgcn_sched_group_barrier(0x2, 24, 0);
#pragma unroll
      for (int i = 0; i < 8; ++i) { __builtin_amdgcn_sched_group_barrier(0x8, 1, 0); __builtin_amdgcn_sched_group_barrier(0x2, 3, 0); }
      *(LAS f4*)(L + lrw) = comb2(lo, a1[0], a2[0], a1[1], a2[1]);
      *(LAS f4*)(L + lrw + 1024) = comb2(lo, a1[2], a2[2], a1[3], a2[3]);
      WG_BARRIER();
      if (w < 2) {
        f4 r[4];
#pragma unroll
        for (int j = 0; j < 4; ++j) r[j] = *(LAS f4*)(L + lrr + j * 2048);
        __builtin_amdgcn_raw_buffer_store_b128(put_tag((r[0] + r[1]) + (r[2] + r[3]), T), ws, od, 0, AUX_SC1 | ((DXNT & 1) ? AUX_NT : 0));
        if (w == 0) STAMP(16);
      }
    }
  }
  STAMP_OUT();
}

extern "C" void kernel_launch(void* const* d_in, const int* in_sizes, int n_in, void* d_out, int out_size, void* d_ws, size_t ws_size,
                              hipStream_t stream) {
  Args a;
  a.x = (const bf16_t*)d_in[0]; a.kc = (const bf16_t*)d_in[1]; a.vc = (const bf16_t*)d_in[2]; a.wq = (const bf16_t*)d_in[3]; a.wo = (const bf16_t*)d_in[4];
  a.wg = (const bf16_t*)d_in[6]; a.wu = (const bf16_t*)d_in[7]; a.wd = (const bf16_t*)d_in[8]; a.out = (bf16_t*)d_out; a.ws = (unsigned char*)d_ws;
  (void)hipFuncSetAttribute((const void*)fused_decode, hipFuncAttributeMaxDynamicSharedMemorySize, LDS_BYTES);
  fused_decode<<<256, 448, LDS_BYTES, stream>>>(a);
}
```
